```python
import torch

def _softmax(x, axis=-1):
    return torch.softmax(x, dim=axis)
def _gelu(x):
    return torch.nn.functional.gelu(x, approximate="tanh")
def _silu(x):
    return torch.nn.functional.silu(x)
def _sigmoid(x):
    return torch.sigmoid(x)
def _layernorm(x, axis=-1, eps=1e-5):
    m = x.mean(dim=axis, keepdim=True)
    v = x.var(dim=axis, keepdim=True, unbiased=False)
    return (x - m) / torch.sqrt(v + eps)
def _scatter_add(tgt, idx, src, axis=0):
    return tgt.clone().index_add_(axis, idx, src)
def _segment_sum(src, seg, n):
    out = torch.zeros((n,) + src.shape[1:], dtype=src.dtype)
    return out.index_add_(0, seg, src)
def _pad(x, pw):
    flat = tuple(v for pair in reversed(pw) for v in pair)
    return torch.nn.functional.pad(x, flat)

kernel_name = 'parallel_sum__grouped_kv_mqa__dense_activation_relu__prompt'

def setup_inputs():

    _w = (lambda a: (lambda t: t.to(torch.bfloat16) if t.is_floating_point() else t)(torch.as_tensor(a)))
    def _A_setup():
        B, S, D, H = 1, 2048, 640, 32
        Dh = D // H
        return {
            "x": _w((lambda *s: torch.randn(*s, dtype=torch.bfloat16) * 0.05)(B, S, D)),
            "wq": _w((lambda *s: torch.randn(*s, dtype=torch.bfloat16) * 0.1)(D, H * Dh)),
            "wk": _w((lambda *s: torch.randn(*s, dtype=torch.bfloat16) * 0.1)(D, Dh)),
            "wv": _w((lambda *s: torch.randn(*s, dtype=torch.bfloat16) * 0.05)(D, Dh)),
            "wo": _w((lambda *s: torch.randn(*s, dtype=torch.bfloat16) * 0.05)(H * Dh, D)),
        }
    _A_in = _A_setup()

    def _B_setup():
        B, S, D, Dff = 1, 2048, 640, 1728
        return {
            "x": _w((lambda *s: torch.randn(*s, dtype=torch.bfloat16) * 0.05)(B, S, D)),
            "w1": _w((lambda *s: torch.randn(*s, dtype=torch.bfloat16) * 0.05)(D, Dff)),
            "w2": _w((lambda *s: torch.randn(*s, dtype=torch.bfloat16) * 0.05)(Dff, D)),
        }
    _B_in = _B_setup()

    return {
        "A_x": _A_in["x"],
        "A_wq": _A_in["wq"],
        "A_wk": _A_in["wk"],
        "A_wv": _A_in["wv"],
        "A_wo": _A_in["wo"],
        "B_x": _B_in["x"],
        "B_w1": _B_in["w1"],
        "B_w2": _B_in["w2"],
    }

def reference(A_x, A_wq, A_wk, A_wv, A_wo, B_x, B_w1, B_w2):
    def _A_ref(x, wq, wk, wv, wo):
        B, S, D = x.shape
        H = 32
        Dh = D // H
        q = (lambda x, *d: x.permute(*d))((x @ wq).reshape(B, S, H, Dh), 0, 2, 1, 3)
        k = x @ wk
        v = x @ wv
        scores = torch.einsum("bhqd,bkd->bhqk", q, k) * (Dh ** -0.5)
        p = _softmax(scores, axis=-1)
        o = torch.einsum("bhqk,bkd->bhqd", p, v)
        return (lambda x, *d: x.permute(*d))(o, 0, 2, 1, 3).reshape(B, S, H * Dh) @ wo

    def _B_ref(x, w1, w2):
        h = torch.einsum("bsd,df->bsf", x, w1)
        zero = torch.zeros_like(h)
        h = torch.maximum(h, zero)
        return torch.einsum("bsf,fd->bsd", h, w2)

    x = A_x
    xn = _layernorm(x, axis=-1)
    return x + _A_ref(xn, A_wq, A_wk, A_wv, A_wo) + _B_ref(xn, B_w1, B_w2)
```

```cpp
#include <hip/hip_runtime.h>
#include <hip/hip_bf16.h>
typedef unsigned short u16;
static __device__ __forceinline__ float b2f(u16 v) { return __uint_as_float(((unsigned)v) << 16); }
static __device__ __forceinline__ u16 f2b(float f) { __hip_bfloat16 h = __float2bfloat16(f); return *(u16*)&h; }
static __device__ __forceinline__ float rb(float f) { return b2f(f2b(f)); }
constexpr int S = 2048, D = 640, H = 32, DH = 20, F = 1728;

__global__ void k_ln(const u16* x, u16* xn) {
  int row = blockIdx.x; int t = threadIdx.x;
  float v[10]; float s = 0;
  for (int i = 0; i < 10; ++i) { v[i] = b2f(x[row * D + t + 64 * i]); s += v[i]; }
  for (int o = 32; o; o >>= 1) s += __shfl_xor(s, o);
  float mean = s / D; float q = 0;
  for (int i = 0; i < 10; ++i) { float d = v[i] - mean; q += d * d; }
  for (int o = 32; o; o >>= 1) q += __shfl_xor(q, o);
  float m = rb(mean), var = rb(q / D); float sd = rb(sqrtf(rb(var + 1e-5f)));
  for (int i = 0; i < 10; ++i) xn[row * D + t + 64 * i] = f2b(rb(v[i] - m) / sd);
}
__global__ void k_gemm(const u16* A, const u16* B, u16* C, int M, int N, int K, int relu) {
  int n = blockIdx.x * 64 + threadIdx.x; int m0 = blockIdx.y * 8;
  if (n >= N) return;
  float acc[8] = {0};
  for (int k = 0; k < K; ++k) { float b = b2f(B[k * N + n]); for (int i = 0; i < 8; ++i) acc[i] += b2f(A[(m0 + i) * K + k]) * b; }
  for (int i = 0; i < 8; ++i) { float r = acc[i]; if (relu) r = fmaxf(r, 0.f); C[(m0 + i) * N + n] = f2b(r); }
}
__global__ void k_attn(const u16* Q, const u16* Kk, const u16* V, u16* O) {
  int idx = blockIdx.x * 64 + threadIdx.x; int row = idx / H, h = idx % H;
  float q[DH], o[DH]; for (int d = 0; d < DH; ++d) { q[d] = b2f(Q[row * D + h * DH + d]); o[d] = 0; }
  float m = -1e30f, l = 0;
  for (int k = 0; k < S; ++k) {
    float s = 0; for (int d = 0; d < DH; ++d) s += q[d] * b2f(Kk[k * DH + d]);
    s *= 0.22360679774997896f;
    float mn = fmaxf(m, s); float a = __expf(m - mn), p = __expf(s - mn); l = l * a + p;
    for (int d = 0; d < DH; ++d) o[d] = o[d] * a + p * b2f(V[k * DH + d]);
    m = mn;
  }
  for (int d = 0; d < DH; ++d) O[row * D + h * DH + d] = f2b(o[d] / l);
}
__global__ void k_fin(const u16* x, const u16* a, const u16* b, u16* out, int n) {
  int i = blockIdx.x * 256 + threadIdx.x; if (i < n) out[i] = f2b(rb(b2f(x[i]) + b2f(a[i])) + b2f(b[i]));
}
extern "C" void kernel_launch(void* const* d_in, const int* in_sizes, int n_in, void* d_out, int out_size, void* d_ws, size_t ws_size, hipStream_t stream) {
  const u16* x = (const u16*)d_in[0]; const u16* wq = (const u16*)d_in[1]; const u16* wk = (const u16*)d_in[2]; const u16* wv = (const u16*)d_in[3];
  const u16* wo = (const u16*)d_in[4]; const u16* w1 = (const u16*)d_in[6]; const u16* w2 = (const u16*)d_in[7];
  u16* ws = (u16*)d_ws; u16* xn = ws; u16* Q = xn + S * D; u16* Kk = Q + S * D; u16* V = Kk + S * 32; u16* O = V + S * 32; u16* Hh = O + S * D; u16* A = Hh + S * F; u16* B = A + S * D;
  k_ln<<<S, 64, 0, stream>>>(x, xn);
  k_gemm<<<dim3(10, S / 8), 64, 0, stream>>>(xn, wq, Q, S, D, D, 0);
  k_gemm<<<dim3(1, S / 8), 64, 0, stream>>>(xn, wk, Kk, S, DH, D, 0);
  k_gemm<<<dim3(1, S / 8), 64, 0, stream>>>(xn, wv, V, S, DH, D, 0);
  k_gemm<<<dim3(27, S / 8), 64, 0, stream>>>(xn, w1, Hh, S, F, D, 1);
  k_attn<<<S * H / 64, 64, 0, stream>>>(Q, Kk, V, O);
  k_gemm<<<dim3(10, S / 8), 64, 0, stream>>>(O, wo, A, S, D, D, 0);
  k_gemm<<<dim3(10, S / 8), 64, 0, stream>>>(Hh, w2, B, S, D, F, 0);
  k_fin<<<S * D / 256, 256, 0, stream>>>(x, A, B, (u16*)d_out, S * D);
}
```

```cpp
#include <hip/hip_runtime.h>
#include <hip/hip_bf16.h>
#include <stdint.h>

typedef unsigned short u16;
typedef unsigned long long u64;
typedef __bf16 bfx2 __attribute__((ext_vector_type(2)));
typedef __bf16 bfx8 __attribute__((ext_vector_type(8)));
typedef short s16x2 __attribute__((ext_vector_type(2)));
typedef short s16x4 __attribute__((ext_vector_type(4)));
typedef float f32x2 __attribute__((ext_vector_type(2)));
typedef float f32x4 __attribute__((ext_vector_type(4)));
typedef float f32x16 __attribute__((ext_vector_type(16)));
typedef unsigned u32x2 __attribute__((ext_vector_type(2)));
typedef unsigned u32x4 __attribute__((ext_vector_type(4)));
#define LAS __attribute__((address_space(3)))
#define GAS __attribute__((address_space(1)))

#define XMAP 1

namespace {
constexpr int S = 2048, D = 640, F = 1728;
constexpr int XP = 1296;
constexpr int L_XN = 0, L_QI = 64 * XP, L_R = L_QI + 12288, L_MISC = L_R + 66560, L_TOTAL = 163840;
constexpr int L_C0 = L_R + 25600, L_C1 = L_C0 + 20480;
constexpr size_t W_EPOCH = 0, W_F1 = 4096, W_F2 = 8192, W_F3 = 16384, W_STAMP = 32768, W_KV = 1 << 20, W_H = 2 << 20, W_O = 10 << 20;
constexpr float QSCALE = 0.22360679774997896f * 1.4426950408889634f;
constexpr unsigned SPIN_CAP = 1u << 18;

struct WoTab { unsigned v[25 * 64]; };
constexpr WoTab make_wotab() {
  WoTab t{};
  for (int s = 0; s < 5; ++s) for (int m = 0; m < 5; ++m) for (int l = 0; l < 64; ++l) {
    int c = 64 * m + l, r = c / 10, pos = c % 10;
    int g = 2 * (r >> 4) + ((r >> 2) & 1), tt = (r >> 3) & 1, e = r & 3;
    int gc = 4 * s + g, blk = gc / 10, ci = gc % 10;
    int row = 80 * blk + (ci < 8 ? 20 * (ci >> 1) + 4 * (ci & 1) + 8 * tt + e : 20 * ((ci - 8) + 2 * tt) + 16 + e);
    t.v[(s * 5 + m) * 64 + l] = row * 1280 + pos * 16;
  }
  return t;
}
__device__ const WoTab wotab = make_wotab();

__device__ __forceinline__ unsigned rfl(unsigned x) { return __builtin_amdgcn_readfirstlane(x); }
__device__ __forceinline__ const char* rflp(const char* p) {
  u64 v = (u64)p; return (const char*)(((u64)rfl((unsigned)(v >> 32)) << 32) | rfl((unsigned)v));
}
__device__ __forceinline__ int fresh_lane() {
  int l; asm volatile("v_mbcnt_lo_u32_b32 %0, -1, 0\n\tv_mbcnt_hi_u32_b32 %0, -1, %0" : "=v"(l)); return l;
}
__device__ __forceinline__ void dma16(const char* sbase, unsigned voff, unsigned lds) {
  unsigned sv;
  asm volatile("s_mov_b32 %0, m0\n\ts_mov_b32 m0, %3\n\ts_nop 0\n\tglobal_load_lds_dwordx4 %1, %2\n\ts_mov_b32 m0, %0"
               : "=&s"(sv) : "v"(voff), "s"(sbase), "s"(lds) : "memory");
}
__device__ __forceinline__ void dma16_sc1(const char* sbase, unsigned voff, unsigned lds) {
  unsigned sv;
  asm volatile("s_mov_b32 %0, m0\n\ts_mov_b32 m0, %3\n\ts_nop 0\n\tglobal_load_lds_dwordx4 %1, %2 sc1\n\ts_mov_b32 m0, %0"
               : "=&s"(sv) : "v"(voff), "s"(sbase), "s"(lds) : "memory");
}
#define VMWAIT(n) asm volatile("s_waitcnt vmcnt(" #n ") lgkmcnt(0)" ::: "memory")
#define BARRIER() asm volatile("s_waitcnt lgkmcnt(0)\n\ts_barrier" ::: "memory")
__device__ __forceinline__ bfx8 ld128(unsigned a) { return *(const LAS bfx8*)(uintptr_t)a; }
__device__ __forceinline__ void st128(unsigned a, f32x4 v) { *(LAS f32x4*)(uintptr_t)a = v; }
typedef short v4i16_t __attribute__((ext_vector_type(4)));
__device__ __forceinline__ bfx8 ldtr(unsigned a0, unsigned a1) {
  v4i16_t lo = __builtin_amdgcn_ds_read_tr16_b64_v4i16((LAS v4i16_t*)(uintptr_t)a0);
  v4i16_t hi = __builtin_amdgcn_ds_read_tr16_b64_v4i16((LAS v4i16_t*)(uintptr_t)a1);
  return __builtin_bit_cast(bfx8, __builtin_shufflevector(lo, hi, 0, 1, 2, 3, 4, 5, 6, 7));
}
__device__ __forceinline__ unsigned pk(float a, float b) { f32x2 v = {a, b}; return __builtin_bit_cast(unsigned, __builtin_convertvector(v, bfx2)); }
__device__ __forceinline__ float rbf(float a) { return __uint_as_float(pk(a, a) << 16); }
#define MFMA16(a, b, c) __builtin_amdgcn_mfma_f32_16x16x32_bf16(a, b, c, 0, 0, 0)
#define MFMA32(a, b, c) __builtin_amdgcn_mfma_f32_32x32x16_bf16(a, b, c, 0, 0, 0)
__device__ __forceinline__ void st_sc1_64(char* p, u64 v) { __hip_atomic_store((GAS u64*)p, v, __ATOMIC_RELAXED, __HIP_MEMORY_SCOPE_AGENT); }
__device__ __forceinline__ u64 ld_sc1_64(const char* p) { return __hip_atomic_load((GAS u64*)p, __ATOMIC_RELAXED, __HIP_MEMORY_SCOPE_AGENT); }
__device__ __forceinline__ void poll(const char* p, bool need, u64 tag) {
  for (unsigned it = 0; it < SPIN_CAP; ++it) {
    u64 v = ld_sc1_64(p);
    if (__all(!need || v == tag)) break;
    __builtin_amdgcn_s_sleep(2);
  }
  asm volatile("" ::: "memory");
}
#define STAMPON 0
#define STAMPT(i) do {} while (0)
#define STAMPC(i) do {} while (0)

__device__ __forceinline__ void attn_core(f32x16 (&o)[8], u32x4 rsrc, const char* flagp, u64 tag, unsigned qlds, unsigned first, u64* dbg) {
  asm volatile(R"ASM(
.macro LOADK b
  s_setprio 3
  buffer_load_dwordx4 v[208+14*\b:211+14*\b], v237, %8, s40 offen sc1
  s_mov_b64 exec, s[44:45]
  buffer_load_dwordx2 v[212+14*\b:213+14*\b], v238, %8, s40 offen offset:1024 sc1
  s_mov_b64 exec, -1
  s_add_u32 s40, s40, 2560
  s_min_u32 s40, s40, 38400
  s_setprio 0
.endm
.macro LOADV b
  s_setprio 3
  s_mov_b64 exec, s[46:47]
  buffer_load_dwordx4 v[214+14*\b:217+14*\b], v239, %8, s41 offen offset:1280 sc1
  buffer_load_dwordx4 v[218+14*\b:221+14*\b], v239, %8, s41 offen offset:1920 sc1
  s_mov_b64 exec, -1
  s_add_u32 s41, s41, 2560
  s_min_u32 s41, s41, 38400
  s_setprio 0
.endm
.macro EXPS X, a, n
  .set ei, \a
  .rept \n
  v_exp_f32 v[\X+ei], v[\X+ei]
  .set ei, ei+1
  .endr
.endm
.macro TILE b, qt
  .set tX, 128+16*(\qt&1)
  .set tY, 128+16*((\qt+1)&1)
  .set qn, 160+6*((\qt+1)&7)
  .set kb, 208+14*((\b+((\qt+1)>>3))&1)
  .set vc, 208+14*\b
  .if \qt == 0
  .set vp, 208+14*(1-\b)
  .else
  .set vp, vc
  .endif
  .set op, 16*((\qt+7)&7)
  .set oc, 16*\qt
  EXPS tX, 0, 2
  v_smfmac_f32_32x32x32_bf16 v[op:op+15], v[vp+10:vp+13], v[tY+8:tY+15], v236
  EXPS tX, 2, 1
  .if \qt == 1
  LOADV (1-\b)
  .endif
  EXPS tX, 3, 2
  .if \qt == 7
  s_waitcnt vmcnt(2)
  .endif
  v_mfma_f32_32x32x8_bf16 v[tY:tY+15], v[kb+4:kb+5], v[qn+4:qn+5], 0
  EXPS tX, 5, 4
  v_mfma_f32_32x32x16_bf16 v[tY:tY+15], v[kb:kb+3], v[qn:qn+3], v[tY:tY+15]
  EXPS tX, 9, 2
  .if \qt == 7
  LOADK \b
  .endif
  EXPS tX, 11, 2
  .if \qt == 0
  s_waitcnt vmcnt(2)
  .endif
  v_smfmac_f32_32x32x32_bf16 v[oc:oc+15], v[vc+6:vc+9], v[tX:tX+7], v236
  EXPS tX, 13, 3
.endm
  .macro TRY id
  s_cmp_lg_u32 s43, 0
  s_cbranch_scc1 Ltry\id\()_%=
  s_waitcnt vmcnt(0)
  v_cmp_eq_u64 vcc, %10, v[132:133]
  v_cmp_eq_u64 s[48:49], %10, v[134:135]
  s_nop 3
  s_and_b64 s[48:49], s[48:49], vcc
  s_cmp_eq_u64 s[48:49], -1
  s_cbranch_scc1 Lok\id\()_%=
  global_load_dwordx4 v[132:135], v237, %9 sc1
  s_branch Ltry\id\()_%=
Lok\id\()_%=:
  LOADK 0
  LOADV 0
  LOADK 1
  s_mov_b32 s43, 1
Ltry\id\()_%=:
  .endm
  v_mbcnt_lo_u32_b32 v236, -1, 0
  v_mbcnt_hi_u32_b32 v236, -1, v236
  v_lshlrev_b32 v237, 4, v236
  global_load_dwordx4 v[132:135], v237, %9 sc1
  v_and_b32 v238, 31, v236
  v_lshrrev_b32 v239, 5, v236
  v_mul_u32_u24 v128, 48, v238
  v_add_u32 v128, %11, v128
  v_lshl_add_u32 v129, v239, 3, v128
  v_lshl_add_u32 v128, v239, 4, v128
  v_mad_u32_u24 v239, v239, 20, v238
  v_lshlrev_b32 v239, 4, v239
  v_cmp_eq_u32 vcc, 20, v238
  v_lshlrev_b32 v238, 3, v236
  v_mov_b32 v130, 0x3f803f80
  v_mov_b32 v131, 0x00003f80
  v_cndmask_b32 v136, 0, v130, vcc
  .set qi, 0
  .rept 8
  v_mov_b32 v[214+qi], v136
  v_mov_b32 v[228+qi], v136
  v_mov_b32 v[152+qi], 0
  .set qi, qi+1
  .endr
  v_mov_b32 v212, v130
  v_mov_b32 v226, v130
  v_mov_b32 v213, v131
  v_mov_b32 v227, v131
  .set qi, 0
  .rept 128
  v_mov_b32 v[qi], 0
  .set qi, qi+1
  .endr
  v_mov_b32 v236, 0xdddd
  s_mov_b32 s44, -1
  s_mov_b32 s45, 0
  s_mov_b32 s46, 0xfffff
  s_mov_b32 s47, 0xfffff
  s_mov_b32 s40, 0
  s_mov_b32 s41, 0
  s_mov_b32 s43, 0
  s_mov_b32 s42, 0x40000
  s_cmp_eq_u32 %12, 0
  s_cbranch_scc1 Lnobar%=
  TRY 0
  s_barrier
  TRY 1
  s_barrier
  TRY 2
  s_barrier
  TRY 3
  s_barrier
Lnobar%=:
  .if %c14
  s_memrealtime s[56:57]
  s_memtime s[58:59]
  s_waitcnt lgkmcnt(0)
  .endif
  .set qi, 0
  .rept 8
  ds_read_b128 v[160+6*qi:163+6*qi], v128 offset:1536*qi
  ds_read_b64 v[164+6*qi:165+6*qi], v129 offset:1536*qi+32
  .set qi, qi+1
  .endr
Lpoll%=:
  TRY 4
  s_cmp_lg_u32 s43, 0
  s_cbranch_scc1 Lgo%=
  s_sleep 2
  s_sub_u32 s42, s42, 1
  s_cmp_lg_u32 s42, 0
  s_cbranch_scc1 Lpoll%=
  s_waitcnt vmcnt(0)
  LOADK 0
  LOADV 0
  LOADK 1
Lgo%=:
  .if %c14
  s_memrealtime s[50:51]
  .endif
  s_waitcnt vmcnt(4) lgkmcnt(0)
  v_mfma_f32_32x32x8_bf16 v[128:143], v[212:213], v[164:165], 0
  v_mfma_f32_32x32x16_bf16 v[128:143], v[208:211], v[160:163], v[128:143]
  s_mov_b32 s42, 8
  s_nop 15
  .if %c14
  s_memrealtime s[52:53]
  .endif
Lloop%=:
  TILE 0, 0
  TILE 0, 1
  TILE 0, 2
  TILE 0, 3
  TILE 0, 4
  TILE 0, 5
  TILE 0, 6
  TILE 0, 7
  TILE 1, 0
  TILE 1, 1
  TILE 1, 2
  TILE 1, 3
  TILE 1, 4
  TILE 1, 5
  TILE 1, 6
  TILE 1, 7
  s_sub_u32 s42, s42, 1
  s_cmp_lg_u32 s42, 0
  s_cbranch_scc1 Lloop%=
  .if %c14
  s_memrealtime s[54:55]
  .endif
  s_nop 1
  v_smfmac_f32_32x32x32_bf16 v[112:127], v[232:235], v[152:159], v236
  s_waitcnt vmcnt(0)
  .if %c14
  s_waitcnt lgkmcnt(0)
  v_mov_b32 v128, 0
  v_mov_b32 v130, s50
  v_mov_b32 v131, s51
  v_mov_b32 v132, s52
  v_mov_b32 v133, s53
  v_mov_b32 v134, s54
  v_mov_b32 v135, s55
  v_mov_b32 v136, s56
  v_mov_b32 v137, s57
  v_mov_b32 v138, s58
  v_mov_b32 v139, s59
  s_mov_b64 exec, 1
  global_store_dwordx4 v128, v[130:133], %13 offset:56
  global_store_dwordx2 v128, v[134:135], %13 offset:72
  global_store_dwordx2 v128, v[136:137], %13 offset:32
  global_store_dwordx2 v128, v[138:139], %13 offset:104
  s_mov_b64 exec, -1
  s_waitcnt vmcnt(0)
  .endif
  s_nop 15
.purgem LOADK
.purgem LOADV
.purgem EXPS
.purgem TILE
.purgem TRY
)ASM"
    : "=&{v[0:15]}"(o[0]), "=&{v[16:31]}"(o[1]), "=&{v[32:47]}"(o[2]), "=&{v[48:63]}"(o[3]),
      "=&{v[64:79]}"(o[4]), "=&{v[80:95]}"(o[5]), "=&{v[96:111]}"(o[6]), "=&{v[112:127]}"(o[7])
    : "s"(rsrc), "s"(flagp), "s"(tag), "s"(qlds), "s"(first), "s"(dbg), "i"(STAMPON)
    : "memory", "vcc", "scc", "s40", "s41", "s42", "s43", "s44", "s45", "s46", "s47", "s48", "s49", "s50", "s51", "s52", "s53", "s54", "s55", "s56", "s57", "s58", "s59",
      "v128", "v129", "v130", "v131", "v132", "v133", "v134", "v135", "v136", "v137", "v138", "v139", "v140", "v141", "v142", "v143",
      "v144", "v145", "v146", "v147", "v148", "v149", "v150", "v151", "v152", "v153", "v154", "v155", "v156", "v157", "v158", "v159",
      "v160", "v161", "v162", "v163", "v164", "v165", "v166", "v167", "v168", "v169", "v170", "v171", "v172", "v173", "v174", "v175",
      "v176", "v177", "v178", "v179", "v180", "v181", "v182", "v183", "v184", "v185", "v186", "v187", "v188", "v189", "v190", "v191",
      "v192", "v193", "v194", "v195", "v196", "v197", "v198", "v199", "v200", "v201", "v202", "v203", "v204", "v205", "v206", "v207",
      "v208", "v209", "v210", "v211", "v212", "v213", "v214", "v215", "v216", "v217", "v218", "v219", "v220", "v221", "v222", "v223",
      "v224", "v225", "v226", "v227", "v228", "v229", "v230", "v231", "v232", "v233", "v234", "v235", "v236", "v237", "v238", "v239");
}

typedef short s16x4v __attribute__((ext_vector_type(4)));

#define PAD1 7
#define PAD2 3
__device__ __forceinline__ void ffn1_core(f32x16 (&acc)[4], u32x4 rsrc, unsigned ring, unsigned vt0, unsigned vt1, unsigned vx, unsigned voff) {
  asm volatile(R"ASM(
.macro GAP nf
  .if %c10 > \nf
  s_nop (%c10-\nf-1)
  .endif
.endm
.macro DMA1 st
  s_add_u32 m0, %5, (\st&7)*2048
  s_add_u32 s41, s40, 26624
  buffer_load_dwordx4 %9, %4, s40 offen lds
  buffer_load_dwordx4 %9, %4, s41 offen offset:1024 lds
  s_add_u32 s40, s40, 55296
.endm
.macro RDA0 st
  ds_read_b64_tr_b16 v[64+16*(\st&1):65+16*(\st&1)], %6 offset:(\st&7)*2048
  ds_read_b64_tr_b16 v[66+16*(\st&1):67+16*(\st&1)], %6 offset:(\st&7)*2048+1024
.endm
.macro RDA1 st
  ds_read_b64_tr_b16 v[68+16*(\st&1):69+16*(\st&1)], %7 offset:(\st&7)*2048
  ds_read_b64_tr_b16 v[70+16*(\st&1):71+16*(\st&1)], %7 offset:(\st&7)*2048+1024
.endm
.macro RDB st
  ds_read_b128 v[72+16*(\st&1):75+16*(\st&1)], %8 offset:\st*32
  ds_read_b128 v[76+16*(\st&1):79+16*(\st&1)], %8 offset:\st*32+41472
.endm
.macro MM d, a, b, st
  .if \st == 0
  v_mfma_f32_32x32x16_bf16 v[\d:\d+15], v[\a:\a+3], v[\b:\b+3], 0
  .else
  v_mfma_f32_32x32x16_bf16 v[\d:\d+15], v[\a:\a+3], v[\b:\b+3], v[\d:\d+15]
  .endif
.endm
.macro STEP st
  .set pb, 64+16*(\st&1)
  .if \st <= 32
  s_waitcnt vmcnt(12) lgkmcnt(2)
  .elseif \st < 39
  s_waitcnt vmcnt(2*(38-\st)) lgkmcnt(2)
  .else
  s_waitcnt lgkmcnt(2)
  .endif
  MM 0, pb, (pb+8), \st
  .if \st < 39
  RDA0 (\st+1)
  .endif
  GAP 2
  MM 16, pb, (pb+12), \st
  .if \st < 39
  RDB (\st+1)
  s_waitcnt lgkmcnt(4)
  .else
  s_waitcnt lgkmcnt(0)
  .endif
  GAP 3
  MM 32, (pb+4), (pb+8), \st
  .if \st < 39
  RDA1 (\st+1)
  .endif
  GAP 2
  MM 48, (pb+4), (pb+12), \st
  .if \st < 32
  DMA1 \st
  GAP 6
  .else
  GAP 1
  .endif
.endm
  s_mov_b32 s42, m0
  s_mov_b32 s40, 0
  .set si, 0
  .rept 8
  DMA1 si
  .set si, si+1
  .endr
  s_waitcnt vmcnt(14)
  RDA0 0
  RDB 0
  RDA1 0
  .set si, 0
  .rept 40
  STEP si
  .set si, si+1
  .endr
  s_mov_b32 m0, s42
  s_nop 15
.purgem GAP
.purgem DMA1
.purgem RDA0
.purgem RDA1
.purgem RDB
.purgem MM
.purgem STEP
)ASM"
    : "=&{v[0:15]}"(acc[0]), "=&{v[16:31]}"(acc[1]), "=&{v[32:47]}"(acc[2]), "=&{v[48:63]}"(acc[3])
    : "s"(rsrc), "s"(ring), "v"(vt0), "v"(vt1), "v"(vx), "v"(voff), "i"(PAD1)
    : "memory", "scc", "s40", "s41", "s42",
      "v64", "v65", "v66", "v67", "v68", "v69", "v70", "v71", "v72", "v73", "v74", "v75", "v76", "v77", "v78", "v79",
      "v80", "v81", "v82", "v83", "v84", "v85", "v86", "v87", "v88", "v89", "v90", "v91", "v92", "v93", "v94", "v95");
}

__device__ __forceinline__ void ffn2_core(f32x16 (&acc)[5], u32x4 rw, u32x4 rh, unsigned ring, unsigned hring, unsigned ns, unsigned vwr, unsigned vhr,
                                          unsigned w0, unsigned w1, unsigned w2, unsigned w3, unsigned w4, unsigned vh) {
  asm volatile(R"ASM(
.macro GAP nf
  .if %c18 > \nf
  s_nop (%c18-\nf-1)
  .endif
.endm
.macro RDW n, hi
  .if (\n) < 70
  ds_read_b64_tr_b16 v[112+4*((\n)%%3)+2*\hi:113+4*((\n)%%3)+2*\hi], %10 offset:(((\n)/5)%%3)*5120+((\n)%%5)*32+1280*\hi
  .endif
.endm
.macro RDH i, t
  .if (\i) < 14
  ds_read_b128 v[80+16*((\i)&1)+4*\t:83+16*((\i)&1)+4*\t], %11 offset:((\i)%%3)*4096+1024*\t
  .endif
.endm
.macro MM i, c, t
  .set md, 16*\c+4*\t
  .set ma, 112+4*((5*\i+\c)%%3)
  .set mb, 80+16*(\i&1)+4*\t
  .if \i == 0
  v_mfma_f32_16x16x32_bf16 v[md:md+3], v[ma:ma+3], v[mb:mb+3], 0
  .else
  v_mfma_f32_16x16x32_bf16 v[md:md+3], v[ma:ma+3], v[mb:mb+3], v[md:md+3]
  .endif
.endm
.macro STEP i
  .set sl, (\i+2)%%3
  s_waitcnt lgkmcnt(2)
  MM \i, 0, 0
  RDW (5*\i+2), 0
  GAP 1
  MM \i, 0, 1
  RDW (5*\i+2), 1
  s_waitcnt lgkmcnt(2)
  GAP 2
  MM \i, 0, 2
  s_add_u32 m0, %7, sl*5120
  s_nop 0
  buffer_load_dwordx4 %12, %5, s40 offen lds
  GAP 3
  MM \i, 0, 3
  buffer_load_dwordx4 %13, %5, s40 offen offset:1024 lds
  buffer_load_dwordx4 %14, %5, s40 offen offset:2048 lds
  GAP 2
  MM \i, 1, 0
  RDW (5*\i+3), 0
  GAP 1
  MM \i, 1, 1
  RDW (5*\i+3), 1
  GAP 1
  MM \i, 1, 2
  buffer_load_dwordx4 %15, %5, s40 offen offset:3072 lds
  s_add_u32 m0, %7, sl*5120+4096
  GAP 2
  MM \i, 1, 3
  buffer_load_dwordx4 %16, %5, s40 offen lds
  s_add_u32 m0, %8, sl*4096
  s_waitcnt lgkmcnt(2)
  GAP 3
  MM \i, 2, 0
  RDW (5*\i+4), 0
  GAP 1
  MM \i, 2, 1
  RDW (5*\i+4), 1
  GAP 1
  MM \i, 2, 2
  buffer_load_dwordx4 %17, %6, s41 offen sc1 lds
  buffer_load_dwordx4 %17, %6, s41 offen offset:1024 sc1 lds
  GAP 2
  MM \i, 2, 3
  buffer_load_dwordx4 %17, %6, s41 offen offset:2048 sc1 lds
  buffer_load_dwordx4 %17, %6, s41 offen offset:3072 sc1 lds
  s_waitcnt vmcnt(9) lgkmcnt(2)
  GAP 3
  MM \i, 3, 0
  RDW (5*\i+5), 0
  s_add_u32 s40, s40, 40960
  GAP 2
  MM \i, 3, 1
  RDW (5*\i+5), 1
  s_add_u32 s41, s41, 4096
  GAP 2
  MM \i, 3, 2
  RDH (\i+1), 0
  GAP 1
  MM \i, 3, 3
  RDH (\i+1), 1
  .if \i < 13
  s_waitcnt lgkmcnt(4)
  .else
  s_waitcnt lgkmcnt(0)
  .endif
  GAP 2
  MM \i, 4, 0
  RDW (5*\i+6), 0
  GAP 1
  MM \i, 4, 1
  RDW (5*\i+6), 1
  GAP 1
  MM \i, 4, 2
  RDH (\i+1), 2
  GAP 1
  MM \i, 4, 3
  RDH (\i+1), 3
  GAP 1
.endm
  s_mov_b32 s42, m0
  s_mov_b32 s40, 81920
  s_mov_b32 s41, 8192
  s_waitcnt vmcnt(4)
  RDW 0, 0
  RDW 0, 1
  RDW 1, 0
  RDW 1, 1
  RDH 0, 0
  RDH 0, 1
  RDH 0, 2
  RDH 0, 3
  .set si, 0
  .rept 13
  STEP si
  .set si, si+1
  .endr
  s_cmp_eq_u32 %9, 13
  s_cbranch_scc1 Lend%=
  STEP 13
Lend%=:
  s_waitcnt vmcnt(0) lgkmcnt(0)
  s_mov_b32 m0, s42
  s_nop 7
.purgem GAP
.purgem RDW
.purgem RDH
.purgem MM
.purgem STEP
)ASM"
    : "=&{v[0:15]}"(acc[0]), "=&{v[16:31]}"(acc[1]), "=&{v[32:47]}"(acc[2]), "=&{v[48:63]}"(acc[3]), "=&{v[64:79]}"(acc[4])
    : "s"(rw), "s"(rh), "s"(ring), "s"(hring), "s"(ns), "v"(vwr), "v"(vhr), "v"(w0), "v"(w1), "v"(w2), "v"(w3), "v"(w4), "v"(vh), "i"(PAD2)
    : "memory", "scc", "s40", "s41", "s42",
      "v80", "v81", "v82", "v83", "v84", "v85", "v86", "v87", "v88", "v89", "v90", "v91", "v92", "v93", "v94", "v95",
      "v96", "v97", "v98", "v99", "v100", "v101", "v102", "v103", "v104", "v105", "v106", "v107", "v108", "v109", "v110", "v111",
      "v112", "v113", "v114", "v115", "v116", "v117", "v118", "v119", "v120", "v121", "v122", "v123");
}
__device__ __forceinline__ u32x4 mk_rsrc(const void* p, unsigned bytes) {
  return (u32x4){rfl((unsigned)(u64)p), rfl((unsigned)((u64)p >> 32)), bytes, 0x00020000u};
}

__global__ __launch_bounds__(512, 2) void fused_kernel(const u16* __restrict__ x, const u16* __restrict__ wq, const u16* __restrict__ wk,
                                                       const u16* __restrict__ wv, const u16* __restrict__ wo, const u16* __restrict__ w1,
                                                       const u16* __restrict__ w2, u16* __restrict__ out, char* __restrict__ ws) {
  extern __shared__ __attribute__((aligned(16))) char smem[];
  const unsigned lb = (unsigned)(uintptr_t)smem;
  const int b = blockIdx.x, xcd = b & 7, idx = b >> 3;
  const int rg = (xcd >> 1) * 8 + (idx >> 2), j = (xcd & 1) * 4 + (idx & 3);
  const int lane = threadIdx.x & 63, w = rfl(threadIdx.x >> 6);
  const int i16 = lane & 15, g4 = lane >> 4, tq = i16 >> 2, tp = lane & 3;
  STAMPT(0);

  u32x4 xv[10];
  u64 ep;
  {
    const char* xb_ = (const char*)x + (size_t)(rg * 64 + w * 8) * 1280;
    const unsigned xo = (lane >> 3) * 1280 + (lane & 7) * 16;
    asm volatile("global_load_dwordx4 %0, %11, %12\n\tglobal_load_dwordx4 %1, %11, %12 offset:128\n\tglobal_load_dwordx4 %2, %11, %12 offset:256\n\t"
                 "global_load_dwordx4 %3, %11, %12 offset:384\n\tglobal_load_dwordx4 %4, %11, %12 offset:512\n\tglobal_load_dwordx4 %5, %11, %12 offset:640\n\t"
                 "global_load_dwordx4 %6, %11, %12 offset:768\n\tglobal_load_dwordx4 %7, %11, %12 offset:896\n\tglobal_load_dwordx4 %8, %11, %12 offset:1024\n\t"
                 "global_load_dwordx4 %9, %11, %12 offset:1152\n\tglobal_load_dwordx2 %10, %13, %14 sc1"
                 : "=&v"(xv[0]), "=&v"(xv[1]), "=&v"(xv[2]), "=&v"(xv[3]), "=&v"(xv[4]), "=&v"(xv[5]), "=&v"(xv[6]), "=&v"(xv[7]), "=&v"(xv[8]), "=&v"(xv[9]), "=&v"(ep)
                 : "v"(xo), "s"(xb_), "v"(0u), "s"(ws + W_EPOCH) : "memory");
  }
  const char* wqb = (const char*)wq + j * 160;
  {
    const char* wkv = (const char*)((j & 1) ? wv : wk);
#pragma unroll
    for (int u = 0; u < 8; ++u) {
      const int p = w + 8 * u;
      const unsigned c = 64 * (p - 25) + lane;
      const bool isw = p < 25;
      dma16(isw ? wkv + p * 1024 : wqb, isw ? lane * 16 : (c / 10) * 1280 + (c % 10) * 16, lb + L_R + p * 1024);
    }
    if (w == 0) { const unsigned c = 64 * 39 + lane; dma16(wqb, (c / 10) * 1280 + (c % 10) * 16, lb + L_R + 64 * 1024); }
  }
  asm volatile("s_waitcnt vmcnt(14)" : "+v"(xv[0]), "+v"(xv[1]), "+v"(xv[2]), "+v"(xv[3]), "+v"(xv[4]) :: "memory");

  {
    float s1 = 0.f, s2 = 0.f, s1b = 0.f, s2b = 0.f;
    const bfx2 one = __builtin_bit_cast(bfx2, 0x3f803f80u);
#pragma unroll
    for (int i = 0; i < 10; ++i) {
      if (i == 5) asm volatile("s_waitcnt vmcnt(8)" : "+v"(xv[5]), "+v"(xv[6]), "+v"(xv[7]), "+v"(xv[8]), "+v"(xv[9]), "+v"(ep), "+v"(s1), "+v"(s2), "+v"(s1b), "+v"(s2b) :: "memory");
#pragma unroll
      for (int e = 0; e < 4; e += 2) {
        const unsigned u0 = xv[i][e], u1 = xv[i][e + 1];
        bfx2 p0 = __builtin_bit_cast(bfx2, u0), p1 = __builtin_bit_cast(bfx2, u1);
        s1 = __builtin_amdgcn_fdot2_f32_bf16(p0, one, s1, false);  s2 = __builtin_amdgcn_fdot2_f32_bf16(p0, p0, s2, false);
        s1b = __builtin_amdgcn_fdot2_f32_bf16(p1, one, s1b, false); s2b = __builtin_amdgcn_fdot2_f32_bf16(p1, p1, s2b, false);
      }
    }
    s1 += s1b; s2 += s2b;
    s1 += __builtin_amdgcn_update_dpp(0.f, s1, 0xB1, 0xf, 0xf, true);  s2 += __builtin_amdgcn_update_dpp(0.f, s2, 0xB1, 0xf, 0xf, true);
    s1 += __builtin_amdgcn_update_dpp(0.f, s1, 0x4E, 0xf, 0xf, true);  s2 += __builtin_amdgcn_update_dpp(0.f, s2, 0x4E, 0xf, 0xf, true);
    s1 += __builtin_amdgcn_update_dpp(0.f, s1, 0x141, 0xf, 0xf, true); s2 += __builtin_amdgcn_update_dpp(0.f, s2, 0x141, 0xf, 0xf, true);
    const float mean = s1 * (1.f / D);
    const float var = fmaxf(s2 * (1.f / D) - mean * mean, 0.f);
    const float m = rbf(mean), sd = rbf(__builtin_sqrtf(rbf(rbf(var) + 1e-5f)));
    const float r = 1.f / sd, mr = -m * r;
    const unsigned xa = lb + L_XN + (w * 8 + (lane >> 3)) * XP + (lane & 4) * 16 + (lane & 2) * 16 + (lane & 1) * 8;
#pragma unroll
    for (int i = 0; i < 10; ++i) {
      u32x4 y;
#pragma unroll
      for (int e = 0; e < 4; ++e) {
        float lo = __uint_as_float(xv[i][e] << 16), hi = __uint_as_float(xv[i][e] & 0xffff0000u);
        y[e] = pk(__builtin_fmaf(lo, r, mr), __builtin_fmaf(hi, r, mr));
      }
      *(LAS u32x2*)(uintptr_t)(xa + i * 128) = (u32x2){y[0], y[1]};
      *(LAS u32x2*)(uintptr_t)(xa + i * 128 + 16) = (u32x2){y[2], y[3]};
    }
    if (threadIdx.x < 256) *(LAS u32x2*)(uintptr_t)(lb + L_QI + threadIdx.x * 48 + 40) = (u32x2){0u, 0u};
    if (threadIdx.x == 256) *(LAS unsigned*)(uintptr_t)(lb + L_MISC) = 0u;
  }
  const u64 tag = (((u64)rfl((unsigned)(ep >> 32)) << 32) | rfl((unsigned)ep)) + 1;
  STAMPT(1);
  VMWAIT(0);
  BARRIER();
  STAMPT(2);

  const unsigned xf16 = lb + L_XN + i16 * XP + g4 * 16;
  const int f0 = 16 * (g4 >> 1) + 4 * (g4 & 1) + tq;
  const unsigned wr = f0 * 160 + tp * 8;

  if (w < 4) {
    const int t16 = rg * 4 + (j >> 1);
    if (w < 2) {
      f32x4 acc = {0.f, 0.f, 0.f, 0.f};
      const unsigned wa = lb + L_R + f0 * 40 + w * 32 + tp * 8, xa = xf16 + (j >> 1) * 16 * XP;
      if (j & 1) {
#pragma unroll
        for (int s = 0; s < 20; ++s) acc = MFMA16(ld128(xa + s * 64), ldtr(wa + s * 1280, wa + s * 1280 + 320), acc);
      } else {
#pragma unroll
        for (int s = 0; s < 20; ++s) acc = MFMA16(ldtr(wa + s * 1280, wa + s * 1280 + 320), ld128(xa + s * 64), acc);
      }
      char* kv = ws + W_KV + (t16 >> 1) * 2560;
      const u64 val = (u64)pk(acc[0], acc[1]) | ((u64)pk(acc[2], acc[3]) << 32);
      if (j & 1) {
        if (w == 0 || i16 < 4) st_sc1_64(kv + 1280 + (((t16 & 1) * 2 + (g4 >> 1)) * 20 + 16 * w + i16) * 16 + (g4 & 1) * 8, val);
      } else {
        const int key = (t16 & 1) * 16 + i16;
        if (w == 0) st_sc1_64(kv + ((g4 >> 1) * 32 + key) * 16 + (g4 & 1) * 8, val);
        else if (g4 == 0) st_sc1_64(kv + 1024 + key * 8, val);
      }
    }
    STAMPT(3);
    BARRIER();
    if (w < 2) {
      VMWAIT(0);
      if (lane == 0) st_sc1_64(ws + W_F1 + ((t16 * 2 + (j & 1)) * 2 + w) * 8, tag);
    }
    const char* kvq = ws + W_KV + w * 40960;
    const u32x4 rsrc = mk_rsrc(kvq, 40960u);
    const char* flagp = rflp(ws + W_F1 + w * 1024);
    const u64 tg = tag;
#pragma nounroll
    for (int attempt = 0;; ++attempt) {
      f32x16 o[8];
      attn_core(o, rsrc, flagp, tg, rfl(lb + L_QI), rfl(attempt == 0 ? 1u : 0u), (u64*)(ws + W_STAMP) + (blockIdx.x * 8 + w) * 16);
      const int lane = fresh_lane(), r32 = lane & 31, h = lane >> 5;
      STAMPT(5); STAMPC(14);
      BARRIER();
#pragma unroll
      for (int qt = 0; qt < 8; ++qt) {
        const int own = qt >> 1;
        if (own != w) {
          const int slot = (w - own - 1) & 3;
          const unsigned a = lb + L_XN + (qt * 3 + slot) * 3072 + lane * 16;
#pragma unroll
          for (int qd = 0; qd < 3; ++qd) st128(a + qd * 1024, (f32x4){o[qt][4 * qd], o[qt][4 * qd + 1], o[qt][4 * qd + 2], o[qt][4 * qd + 3]});
        }
      }
      f32x4 t[2][3];
#pragma unroll
      for (int ww = 0; ww < 4; ++ww) if (ww == 0 || ww == w) {
#pragma unroll
        for (int u = 0; u < 2; ++u)
#pragma unroll
          for (int qd = 0; qd < 3; ++qd) t[u][qd] = (f32x4){o[2 * ww + u][4 * qd], o[2 * ww + u][4 * qd + 1], o[2 * ww + u][4 * qd + 2], o[2 * ww + u][4 * qd + 3]};
      }
      BARRIER();
      bool bad = false;
#pragma unroll
      for (int u = 0; u < 2; ++u) {
        f32x4 acc[3];
#pragma unroll
        for (int qd = 0; qd < 3; ++qd) {
          const unsigned a = lb + L_XN + ((2 * w + u) * 3) * 3072 + qd * 1024 + lane * 16;
          f32x4 p0 = *(const LAS f32x4*)(uintptr_t)a, p1 = *(const LAS f32x4*)(uintptr_t)(a + 3072), p2 = *(const LAS f32x4*)(uintptr_t)(a + 6144);
          acc[qd] = (t[u][qd] + p0) + (p1 + p2);
        }
        auto sw = __builtin_amdgcn_permlane32_swap(__float_as_uint(acc[2][0]), __float_as_uint(acc[2][0]), false, false);
        const float l = __uint_as_float(sw[1]);
        bad |= !(l >= 0x1p-64f && l <= 0x1p100f);
        const float rl = 1.f / l;
        const int rt16 = 2 * u + (r32 >> 4);
        char* ob = ws + W_O + (size_t)rg * 81920 + rt16 * 1024 + (r32 & 15) * 16;
        const int gc = 10 * j + 2 * w + h;
        const u32x4 v = {pk(acc[0][0] * rl, acc[0][1] * rl), pk(acc[0][2] * rl, acc[0][3] * rl), pk(acc[1][0] * rl, acc[1][1] * rl), pk(acc[1][2] * rl, acc[1][3] * rl)};
        char* p = ob + (gc >> 2) * 4096 + (gc & 3) * 256;
        asm volatile("global_store_dwordx4 %0, %1, off sc1\n\ts_nop 1" :: "v"(p), "v"(v) : "memory");
        const int gl = 10 * j + 8 + (w & 1);
        if (h == 0) st_sc1_64(ob + (gl >> 2) * 4096 + (gl & 3) * 256 + (w >> 1) * 8,
                              (u64)pk(acc[2][0] * rl, acc[2][1] * rl) | ((u64)pk(acc[2][2] * rl, acc[2][3] * rl) << 32));
      }
      if (attempt == 0 && __any(bad) && lane == 0) *(volatile LAS unsigned*)(uintptr_t)(lb + L_MISC) = 1u;
      VMWAIT(0);
      BARRIER();
      const unsigned redo = attempt == 0 ? rfl(*(volatile LAS unsigned*)(uintptr_t)(lb + L_MISC)) : 0u;
      if (!redo) break;
      {
        const char* kq = kvq;
        for (int qt = 0; qt < 8; ++qt) {
          const unsigned qa_ = lb + L_QI + qt * 1536 + r32 * 48;
          const bfx8 q1 = ld128(qa_ + h * 16);
          u32x2 q2u = *(const LAS u32x2*)(uintptr_t)(qa_ + 32);
          if (h) q2u = (u32x2){0u, 0u};
          float mx = -__builtin_inff();
          for (int kt = 0; kt < 16; ++kt) {
            const char* tp_ = kq + kt * 2560;
            u32x4 k1 = {0u, 0u, 0u, 0u};
            u64 a0 = ld_sc1_64(tp_ + lane * 16), a1 = ld_sc1_64(tp_ + lane * 16 + 8), a2 = ld_sc1_64(tp_ + 1024 + r32 * 8);
            k1 = (u32x4){(unsigned)a0, (unsigned)(a0 >> 32), (unsigned)a1, (unsigned)(a1 >> 32)};
            u32x2 k2 = {(unsigned)a2, (unsigned)(a2 >> 32)};
            f32x16 sc = {};
            sc = __builtin_amdgcn_mfma_f32_32x32x8bf16_1k(__builtin_bit_cast(s16x4v, k2), __builtin_bit_cast(s16x4v, q2u), sc, 0, 0, 0);
            sc = MFMA32(__builtin_bit_cast(bfx8, k1), q1, sc);
#pragma unroll
            for (int e = 0; e < 16; ++e) mx = fmaxf(mx, sc[e]);
          }
          auto s2 = __builtin_amdgcn_permlane32_swap(__float_as_uint(mx), __float_as_uint(mx), false, false);
          mx = fmaxf(__uint_as_float(s2[0]), __uint_as_float(s2[1]));
          if (h == 0) *(LAS float*)(uintptr_t)(lb + L_XN + ((w * 8 + qt) * 32 + r32) * 4) = mx;
        }
      }
      BARRIER();
      {
        const int q = w * 64 + lane;
        float mx = -__builtin_inff();
#pragma unroll
        for (int ww = 0; ww < 4; ++ww) mx = fmaxf(mx, *(const LAS float*)(uintptr_t)(lb + L_XN + (ww * 256 + q) * 4));
        const float m0 = rbf(mx), m1 = rbf(mx - m0), m2 = rbf(mx - m0 - m1);
        *(LAS u32x2*)(uintptr_t)(lb + L_QI + q * 48 + 40) = (u32x2){pk(-m0, -m1), pk(-m2, 0.f)};
      }
      BARRIER();
    }
    const int lane = fresh_lane();
    if (w == 0 && lane == 0) st_sc1_64(ws + W_F3 + rg * 128 + j * 8, tg);
    if (b == 0 && w == 1 && lane == 0) st_sc1_64(ws + W_EPOCH, tg);
    STAMPT(6);
    BARRIER();
  } else {
    const int gw = w - 4;
    const unsigned ring = lb + L_R + gw * 16384;
    const int r32 = lane & 31, h = lane >> 5;
    unsigned woff[5];
#pragma unroll
    for (int m = 0; m < 5; ++m) { const unsigned c = 64 * m + lane; woff[m] = (c / 10) * 1280 + (c % 10) * 16; }
    {
      f32x4 qa[5];
#pragma unroll
      for (int t = 0; t < 5; ++t) qa[t] = (f32x4){0.f, 0.f, 0.f, 0.f};
      auto q_steps = [&](int chunk, unsigned cb) {
#pragma unroll
        for (int s = 0; s < 4; ++s) {
          const bfx8 xf = ld128(xf16 + gw * 16 * XP + (chunk * 4 + s) * 64);
#pragma unroll
          for (int t = 0; t < 5; ++t) { const unsigned a = cb + wr + s * 5120 + t * 32; qa[t] = MFMA16(ldtr(a, a + 1280), xf, qa[t]); }
        }
      };
      q_steps(0, lb + L_C0);
      q_steps(1, lb + L_C1);
      STAMPT(3);
      BARRIER();
#pragma unroll
      for (int ch = 2; ch < 5; ++ch)
#pragma unroll
        for (int m = 0; m < 5; ++m)
          dma16(wqb + (size_t)(ch * 128 + gw * 32) * 1280, woff[m], lb + (ch == 2 ? L_R : ch == 3 ? L_C0 : L_C1) + (gw * 5 + m) * 1024);
      VMWAIT(10); BARRIER();
      q_steps(2, lb + L_R);
      VMWAIT(5); BARRIER();
      q_steps(3, lb + L_C0);
      VMWAIT(0); BARRIER();
      q_steps(4, lb + L_C1);
#pragma unroll
      for (int t = 0; t < 5; ++t) {
        const int f = 16 * t + 4 * g4, head = f / 20, d = f % 20;
        *(LAS u32x2*)(uintptr_t)(lb + L_QI + (head * 64 + gw * 16 + i16) * 48 + d * 2) =
            (u32x2){pk(qa[t][0] * QSCALE, qa[t][1] * QSCALE), pk(qa[t][2] * QSCALE, qa[t][3] * QSCALE)};
      }
      BARRIER();
    }
    STAMPT(4); STAMPC(13);
    __builtin_amdgcn_s_setprio(1);
    const int nun = j < 3 ? 4 : 3, unit = (j < 3 ? 4 * j : 12 + 3 * (j - 3)) + gw;
    char* hbase = ws + W_H + (size_t)rg * 54 * 4096;
    const unsigned hring = lb + L_XN + gw * 20480;
    const char* wob = (const char*)wo + (size_t)gw * 160 * 1280 + j * 160;
    unsigned to[25];
#pragma unroll
    for (int i = 0; i < 25; ++i) to[i] = wotab.v[i * 64 + lane];
    if (gw < nun) {
      const unsigned voff = (lane >> 3) * 3456 + (((lane & 7) ^ (((lane >> 4) & 1) << 2)) * 16);
      const unsigned tb = ring + (4 * (g4 >> 1) + tq) * 128 + (tp & 1) * 8;
      const unsigned tr0 = tb + ((2 * (g4 & 1) + (tp >> 1)) + 4 * (tq >> 1)) * 16, tr1 = tb + ((2 * (g4 & 1) + (tp >> 1)) + 4 * (1 - (tq >> 1))) * 16;
      f32x16 acc[4];
      ffn1_core(acc, mk_rsrc((const char*)w1 + unit * 128, 640 * 3456), rfl(ring), tr0, tr1, lb + L_XN + r32 * XP + h * 16, voff);
#pragma unroll
      for (int i = 0; i < 25; ++i) asm volatile("" : "+v"(to[i]));
#pragma unroll
      for (int nt = 0; nt < 2; ++nt)
#pragma unroll
        for (int rt = 0; rt < 2; ++rt)
#pragma unroll
          for (int c = 0; c < 2; ++c) {
            u32x4 v;
#pragma unroll
            for (int e = 0; e < 4; ++e)
              v[e] = __builtin_bit_cast(unsigned, __builtin_elementwise_max(__builtin_bit_cast(s16x2, pk(acc[2 * nt + rt][8 * c + 2 * e], acc[2 * nt + rt][8 * c + 2 * e + 1])), (s16x2){0, 0}));
            char* p = hbase + (((2 * unit + nt) * 4 + 2 * rt + (r32 >> 4)) * 4 + 2 * c + h) * 256 + (r32 & 15) * 16;
            asm volatile("global_store_dwordx4 %0, %1, off sc1\n\ts_nop 1" :: "v"(p), "v"(v) : "memory");
          }
    }
    STAMPT(5);
    bfx8 wof[5][5];
#pragma unroll
    for (int s = 0; s < 3; ++s)
#pragma unroll
      for (int m = 0; m < 5; ++m) dma16(wob, to[s * 5 + m], ring + s * 5120 + m * 1024);
    VMWAIT(15);
    if (gw < nun && lane == 0) st_sc1_64(ws + W_F2 + rg * 256 + unit * 8, tag);
    VMWAIT(0);
#pragma unroll
    for (int s = 0; s < 3; ++s)
#pragma unroll
      for (int c = 0; c < 5; ++c) { const unsigned a = ring + s * 5120 + wr + c * 32; wof[s][c] = ldtr(a, a + 1280); }
    VMWAIT(0);
#pragma unroll
    for (int s = 3; s < 5; ++s)
#pragma unroll
      for (int m = 0; m < 5; ++m) dma16(wob, to[s * 5 + m], ring + (s - 3) * 5120 + m * 1024);
    VMWAIT(0);
#pragma unroll
    for (int s = 3; s < 5; ++s)
#pragma unroll
      for (int c = 0; c < 5; ++c) { const unsigned a = ring + (s - 3) * 5120 + wr + c * 32; wof[s][c] = ldtr(a, a + 1280); }
    VMWAIT(0);
    STAMPT(7);
    const int s0 = gw < 2 ? 14 * gw : 28 + 13 * (gw - 2), ns = gw < 2 ? 14 : 13;
    const char* w2b = (const char*)w2 + j * 160 + (size_t)s0 * 32 * 1280;
    const char* hb = hbase + s0 * 4096;
#pragma unroll
    for (int i = 0; i < 2; ++i)
#pragma unroll
      for (int m = 0; m < 5; ++m) dma16(w2b + (size_t)i * 32 * 1280, woff[m], ring + i * 5120 + m * 1024);
    poll(ws + W_F2 + rg * 256 + (lane < 27 ? lane : 0) * 8, lane < 27, tag);
    VMWAIT(0);
    STAMPT(6);
#pragma unroll
    for (int i = 0; i < 2; ++i)
#pragma unroll
      for (int m = 0; m < 4; ++m) dma16_sc1(hb + i * 4096 + m * 1024, lane * 16, hring + i * 4096 + m * 1024);
    f32x16 a5[5];
    ffn2_core(a5, mk_rsrc(w2b, ns * 40960), mk_rsrc(hb, ns * 4096), rfl(ring), rfl(hring), rfl(ns), ring + wr, hring + lane * 16,
              woff[0], woff[1] - 1024, woff[2] - 2048, woff[3] - 3072, woff[4], lane * 16);
    f32x4 acc[5][4];
#pragma unroll
    for (int c = 0; c < 5; ++c)
#pragma unroll
      for (int t = 0; t < 4; ++t) acc[c][t] = (f32x4){a5[c][4 * t], a5[c][4 * t + 1], a5[c][4 * t + 2], a5[c][4 * t + 3]};
    u32x2 xres[5];
    const size_t orow = (size_t)(rg * 64 + gw * 16 + i16) * D + j * 80 + g4 * 4;
#pragma unroll
    for (int c = 0; c < 5; ++c) xres[c] = *(const u32x2*)(x + orow + c * 16);
    STAMPT(8); STAMPC(14);
    BARRIER();
    BARRIER();
    BARRIER();
    if (rfl(*(volatile LAS unsigned*)(uintptr_t)(lb + L_MISC))) { BARRIER(); BARRIER(); BARRIER(); BARRIER(); BARRIER(); }
    STAMPT(9);
    poll(ws + W_F3 + rg * 128 + (2 * gw + (lane & 1)) * 8, true, tag);
    STAMPT(10);
    {
      const auto orsrc = __builtin_amdgcn_make_buffer_rsrc((void*)rflp(ws + W_O + (size_t)rg * 81920 + gw * 20480), 0, 20480, 0x00020000);
      u32x4 of[5][4];
#pragma unroll
      for (int s = 0; s < 5; ++s)
#pragma unroll
        for (int t = 0; t < 4; ++t) of[s][t] = __builtin_amdgcn_raw_buffer_load_b128(orsrc, lane * 16 + (s * 4 + t) * 1024, 0, 16);
#pragma unroll
      for (int s = 0; s < 5; ++s)
#pragma unroll
        for (int c = 0; c < 5; ++c)
#pragma unroll
          for (int t = 0; t < 4; ++t) acc[c][t] = MFMA16(wof[s][c], __builtin_bit_cast(bfx8, of[s][t]), acc[c][t]);
    }
    STAMPT(11);
#pragma unroll
    for (int t = 0; t < 4; ++t) if (t != gw) {
      const int sl = (gw - t - 1) & 3;
#pragma unroll
      for (int c = 0; c < 5; ++c) st128(lb + L_R + ((t * 3 + sl) * 5 + c) * 1024 + lane * 16, acc[c][t]);
    }
    f32x4 mine[5];
#pragma unroll
    for (int t = 0; t < 4; ++t) if (t == 0 || t == gw) {
#pragma unroll
      for (int c = 0; c < 5; ++c) mine[c] = acc[c][t];
    }
    BARRIER();
#pragma unroll
    for (int c = 0; c < 5; ++c) {
      const unsigned a = lb + L_R + (gw * 15 + c) * 1024 + lane * 16;
      f32x4 p0 = *(const LAS f32x4*)(uintptr_t)a, p1 = *(const LAS f32x4*)(uintptr_t)(a + 5120), p2 = *(const LAS f32x4*)(uintptr_t)(a + 10240);
      f32x4 v = (mine[c] + p0) + (p1 + p2);
      v[0] += __uint_as_float(xres[c][0] << 16); v[1] += __uint_as_float(xres[c][0] & 0xffff0000u);
      v[2] += __uint_as_float(xres[c][1] << 16); v[3] += __uint_as_float(xres[c][1] & 0xffff0000u);
      *(u32x2*)(out + orow + c * 16) = (u32x2){pk(v[0], v[1]), pk(v[2], v[3])};
    }
    STAMPT(12);
  }
}
}

extern "C" void kernel_launch(void* const* d_in, const int* in_sizes, int n_in, void* d_out, int out_size, void* d_ws, size_t ws_size,
                              hipStream_t stream) {
  (void)in_sizes; (void)n_in; (void)out_size; (void)ws_size;
  (void)hipFuncSetAttribute((const void*)fused_kernel, hipFuncAttributeMaxDynamicSharedMemorySize, L_TOTAL);
  fused_kernel<<<256, 512, L_TOTAL, stream>>>((const u16*)d_in[0], (const u16*)d_in[1], (const u16*)d_in[2], (const u16*)d_in[3],
                                              (const u16*)d_in[4], (const u16*)d_in[6], (const u16*)d_in[7], (u16*)d_out, (char*)d_ws);
}
```

```cpp
#define ENC_W 4
#define ENC_I 2
#define ENC_J 0
#define ENC_MUL 10
#define ENC_DIV 1
#include <hip/hip_runtime.h>
#include <hip/hip_bf16.h>
#include <stdint.h>

typedef unsigned short u16;
typedef unsigned long long u64;
typedef __bf16 bfx2 __attribute__((ext_vector_type(2)));
typedef __bf16 bfx8 __attribute__((ext_vector_type(8)));
typedef short s16x2 __attribute__((ext_vector_type(2)));
typedef short s16x4 __attribute__((ext_vector_type(4)));
typedef float f32x2 __attribute__((ext_vector_type(2)));
typedef float f32x4 __attribute__((ext_vector_type(4)));
typedef float f32x16 __attribute__((ext_vector_type(16)));
typedef unsigned u32x2 __attribute__((ext_vector_type(2)));
typedef unsigned u32x4 __attribute__((ext_vector_type(4)));
#define LAS __attribute__((address_space(3)))
#define GAS __attribute__((address_space(1)))

#define XMAP 1

namespace {
constexpr int S = 2048, D = 640, F = 1728;
constexpr int XP = 1296;
constexpr int L_XN = 0, L_QI = 64 * XP, L_R = L_QI + 12288, L_MISC = L_R + 66560, L_TOTAL = 163840;
constexpr int L_C0 = L_R + 25600, L_C1 = L_C0 + 20480;
constexpr size_t W_EPOCH = 0, W_F1 = 4096, W_F2 = 8192, W_F3 = 16384, W_STAMP = 32768, W_KV = 1 << 20, W_H = 2 << 20, W_O = 10 << 20;
constexpr float QSCALE = 0.22360679774997896f * 1.4426950408889634f;
constexpr unsigned SPIN_CAP = 1u << 18;

struct WoTab { unsigned v[25 * 64]; };
constexpr WoTab make_wotab() {
  WoTab t{};
  for (int s = 0; s < 5; ++s) for (int m = 0; m < 5; ++m) for (int l = 0; l < 64; ++l) {
    int c = 64 * m + l, r = c / 10, pos = c % 10;
    int g = 2 * (r >> 4) + ((r >> 2) & 1), tt = (r >> 3) & 1, e = r & 3;
    int gc = 4 * s + g, blk = gc / 10, ci = gc % 10;
    int row = 80 * blk + (ci < 8 ? 20 * (ci >> 1) + 4 * (ci & 1) + 8 * tt + e : 20 * ((ci - 8) + 2 * tt) + 16 + e);
    t.v[(s * 5 + m) * 64 + l] = row * 1280 + pos * 16;
  }
  return t;
}
__device__ const WoTab wotab = make_wotab();

__device__ __forceinline__ unsigned rfl(unsigned x) { return __builtin_amdgcn_readfirstlane(x); }
__device__ __forceinline__ const char* rflp(const char* p) {
  u64 v = (u64)p; return (const char*)(((u64)rfl((unsigned)(v >> 32)) << 32) | rfl((unsigned)v));
}
__device__ __forceinline__ int fresh_lane() {
  int l; asm volatile("v_mbcnt_lo_u32_b32 %0, -1, 0\n\tv_mbcnt_hi_u32_b32 %0, -1, %0" : "=v"(l)); return l;
}
__device__ __forceinline__ void dma16(const char* sbase, unsigned voff, unsigned lds) {
  unsigned sv;
  asm volatile("s_mov_b32 %0, m0\n\ts_mov_b32 m0, %3\n\ts_nop 0\n\tglobal_load_lds_dwordx4 %1, %2\n\ts_mov_b32 m0, %0"
               : "=&s"(sv) : "v"(voff), "s"(sbase), "s"(lds) : "memory");
}
__device__ __forceinline__ void dma16_sc1(const char* sbase, unsigned voff, unsigned lds) {
  unsigned sv;
  asm volatile("s_mov_b32 %0, m0\n\ts_mov_b32 m0, %3\n\ts_nop 0\n\tglobal_load_lds_dwordx4 %1, %2 sc1\n\ts_mov_b32 m0, %0"
               : "=&s"(sv) : "v"(voff), "s"(sbase), "s"(lds) : "memory");
}
#define VMWAIT(n) asm volatile("s_waitcnt vmcnt(" #n ") lgkmcnt(0)" ::: "memory")
#define BARRIER() asm volatile("s_waitcnt lgkmcnt(0)\n\ts_barrier" ::: "memory")
__device__ __forceinline__ bfx8 ld128(unsigned a) { return *(const LAS bfx8*)(uintptr_t)a; }
__device__ __forceinline__ void st128(unsigned a, f32x4 v) { *(LAS f32x4*)(uintptr_t)a = v; }
typedef short v4i16_t __attribute__((ext_vector_type(4)));
__device__ __forceinline__ bfx8 ldtr(unsigned a0, unsigned a1) {
  v4i16_t lo = __builtin_amdgcn_ds_read_tr16_b64_v4i16((LAS v4i16_t*)(uintptr_t)a0);
  v4i16_t hi = __builtin_amdgcn_ds_read_tr16_b64_v4i16((LAS v4i16_t*)(uintptr_t)a1);
  return __builtin_bit_cast(bfx8, __builtin_shufflevector(lo, hi, 0, 1, 2, 3, 4, 5, 6, 7));
}
__device__ __forceinline__ unsigned pk(float a, float b) { f32x2 v = {a, b}; return __builtin_bit_cast(unsigned, __builtin_convertvector(v, bfx2)); }
__device__ __forceinline__ float rbf(float a) { return __uint_as_float(pk(a, a) << 16); }
#define MFMA16(a, b, c) __builtin_amdgcn_mfma_f32_16x16x32_bf16(a, b, c, 0, 0, 0)
#define MFMA32(a, b, c) __builtin_amdgcn_mfma_f32_32x32x16_bf16(a, b, c, 0, 0, 0)
__device__ __forceinline__ void st_sc1_64(char* p, u64 v) { __hip_atomic_store((GAS u64*)p, v, __ATOMIC_RELAXED, __HIP_MEMORY_SCOPE_AGENT); }
__device__ __forceinline__ u64 ld_sc1_64(const char* p) { return __hip_atomic_load((GAS u64*)p, __ATOMIC_RELAXED, __HIP_MEMORY_SCOPE_AGENT); }
__device__ __forceinline__ void poll(const char* p, bool need, u64 tag) {
  for (unsigned it = 0; it < SPIN_CAP; ++it) {
    u64 v = ld_sc1_64(p);
    if (__all(!need || v == tag)) break;
    __builtin_amdgcn_s_sleep(2);
  }
  asm volatile("" ::: "memory");
}
#define STAMPON 0
#define STAMPT(i) do { if (fresh_lane() == 0) *(volatile LAS unsigned*)(uintptr_t)(L_MISC + 64 + (w * 16 + (i)) * 4) = (unsigned)__builtin_amdgcn_s_memrealtime(); } while (0)
#define STAMPC(i) do { if (fresh_lane() == 0) *(volatile LAS unsigned*)(uintptr_t)(L_MISC + 64 + (w * 16 + (i)) * 4) = (unsigned)__builtin_amdgcn_s_memtime(); } while (0)

__device__ __forceinline__ void attn_core(f32x16 (&o)[8], u32x4 rsrc, const char* flagp, u64 tag, unsigned qlds, unsigned first, u64* dbg) {
  asm volatile(R"ASM(
.macro LOADK b
  s_setprio 3
  buffer_load_dwordx4 v[208+14*\b:211+14*\b], v237, %8, s40 offen sc1
  s_mov_b64 exec, s[44:45]
  buffer_load_dwordx2 v[212+14*\b:213+14*\b], v238, %8, s40 offen offset:1024 sc1
  s_mov_b64 exec, -1
  s_add_u32 s40, s40, 2560
  s_min_u32 s40, s40, 38400
  s_setprio 0
.endm
.macro LOADV b
  s_setprio 3
  s_mov_b64 exec, s[46:47]
  buffer_load_dwordx4 v[214+14*\b:217+14*\b], v239, %8, s41 offen offset:1280 sc1
  buffer_load_dwordx4 v[218+14*\b:221+14*\b], v239, %8, s41 offen offset:1920 sc1
  s_mov_b64 exec, -1
  s_add_u32 s41, s41, 2560
  s_min_u32 s41, s41, 38400
  s_setprio 0
.endm
.macro EXPS X, a, n
  .set ei, \a
  .rept \n
  v_exp_f32 v[\X+ei], v[\X+ei]
  .set ei, ei+1
  .endr
.endm
.macro TILE b, qt
  .set tX, 128+16*(\qt&1)
  .set tY, 128+16*((\qt+1)&1)
  .set qn, 160+6*((\qt+1)&7)
  .set kb, 208+14*((\b+((\qt+1)>>3))&1)
  .set vc, 208+14*\b
  .if \qt == 0
  .set vp, 208+14*(1-\b)
  .else
  .set vp, vc
  .endif
  .set op, 16*((\qt+7)&7)
  .set oc, 16*\qt
  EXPS tX, 0, 2
  v_smfmac_f32_32x32x32_bf16 v[op:op+15], v[vp+10:vp+13], v[tY+8:tY+15], v236
  EXPS tX, 2, 1
  .if \qt == 1
  LOADV (1-\b)
  .endif
  EXPS tX, 3, 2
  .if \qt == 7
  s_waitcnt vmcnt(2)
  .endif
  v_mfma_f32_32x32x8_bf16 v[tY:tY+15], v[kb+4:kb+5], v[qn+4:qn+5], 0
  EXPS tX, 5, 4
  v_mfma_f32_32x32x16_bf16 v[tY:tY+15], v[kb:kb+3], v[qn:qn+3], v[tY:tY+15]
  EXPS tX, 9, 2
  .if \qt == 7
  LOADK \b
  .endif
  EXPS tX, 11, 2
  .if \qt == 0
  s_waitcnt vmcnt(2)
  .endif
  v_smfmac_f32_32x32x32_bf16 v[oc:oc+15], v[vc+6:vc+9], v[tX:tX+7], v236
  EXPS tX, 13, 3
.endm
  .macro TRY id
  s_cmp_lg_u32 s43, 0
  s_cbranch_scc1 Ltry\id\()_%=
  s_waitcnt vmcnt(0)
  v_cmp_eq_u64 vcc, %10, v[132:133]
  v_cmp_eq_u64 s[48:49], %10, v[134:135]
  s_nop 3
  s_and_b64 s[48:49], s[48:49], vcc
  s_cmp_eq_u64 s[48:49], -1
  s_cbranch_scc1 Lok\id\()_%=
  global_load_dwordx4 v[132:135], v237, %9 sc1
  s_branch Ltry\id\()_%=
Lok\id\()_%=:
  LOADK 0
  LOADV 0
  LOADK 1
  s_mov_b32 s43, 1
Ltry\id\()_%=:
  .endm
  v_mbcnt_lo_u32_b32 v236, -1, 0
  v_mbcnt_hi_u32_b32 v236, -1, v236
  v_lshlrev_b32 v237, 4, v236
  global_load_dwordx4 v[132:135], v237, %9 sc1
  v_and_b32 v238, 31, v236
  v_lshrrev_b32 v239, 5, v236
  v_mul_u32_u24 v128, 48, v238
  v_add_u32 v128, %11, v128
  v_lshl_add_u32 v129, v239, 3, v128
  v_lshl_add_u32 v128, v239, 4, v128
  v_mad_u32_u24 v239, v239, 20, v238
  v_lshlrev_b32 v239, 4, v239
  v_cmp_eq_u32 vcc, 20, v238
  v_lshlrev_b32 v238, 3, v236
  v_mov_b32 v130, 0x3f803f80
  v_mov_b32 v131, 0x00003f80
  v_cndmask_b32 v136, 0, v130, vcc
  .set qi, 0
  .rept 8
  v_mov_b32 v[214+qi], v136
  v_mov_b32 v[228+qi], v136
  v_mov_b32 v[152+qi], 0
  .set qi, qi+1
  .endr
  v_mov_b32 v212, v130
  v_mov_b32 v226, v130
  v_mov_b32 v213, v131
  v_mov_b32 v227, v131
  .set qi, 0
  .rept 128
  v_mov_b32 v[qi], 0
  .set qi, qi+1
  .endr
  v_mov_b32 v236, 0xdddd
  s_mov_b32 s44, -1
  s_mov_b32 s45, 0
  s_mov_b32 s46, 0xfffff
  s_mov_b32 s47, 0xfffff
  s_mov_b32 s40, 0
  s_mov_b32 s41, 0
  s_mov_b32 s43, 0
  s_mov_b32 s42, 0x40000
  s_cmp_eq_u32 %12, 0
  s_cbranch_scc1 Lnobar%=
  TRY 0
  s_barrier
  TRY 1
  s_barrier
  TRY 2
  s_barrier
  TRY 3
  s_barrier
Lnobar%=:
  .if %c14
  s_memrealtime s[56:57]
  s_memtime s[58:59]
  s_waitcnt lgkmcnt(0)
  .endif
  .set qi, 0
  .rept 8
  ds_read_b128 v[160+6*qi:163+6*qi], v128 offset:1536*qi
  ds_read_b64 v[164+6*qi:165+6*qi], v129 offset:1536*qi+32
  .set qi, qi+1
  .endr
Lpoll%=:
  TRY 4
  s_cmp_lg_u32 s43, 0
  s_cbranch_scc1 Lgo%=
  s_sleep 2
  s_sub_u32 s42, s42, 1
  s_cmp_lg_u32 s42, 0
  s_cbranch_scc1 Lpoll%=
  s_waitcnt vmcnt(0)
  LOADK 0
  LOADV 0
  LOADK 1
Lgo%=:
  .if %c14
  s_memrealtime s[50:51]
  .endif
  s_waitcnt vmcnt(4) lgkmcnt(0)
  v_mfma_f32_32x32x8_bf16 v[128:143], v[212:213], v[164:165], 0
  v_mfma_f32_32x32x16_bf16 v[128:143], v[208:211], v[160:163], v[128:143]
  s_mov_b32 s42, 8
  s_nop 15
  .if %c14
  s_memrealtime s[52:53]
  .endif
Lloop%=:
  TILE 0, 0
  TILE 0, 1
  TILE 0, 2
  TILE 0, 3
  TILE 0, 4
  TILE 0, 5
  TILE 0, 6
  TILE 0, 7
  TILE 1, 0
  TILE 1, 1
  TILE 1, 2
  TILE 1, 3
  TILE 1, 4
  TILE 1, 5
  TILE 1, 6
  TILE 1, 7
  s_sub_u32 s42, s42, 1
  s_cmp_lg_u32 s42, 0
  s_cbranch_scc1 Lloop%=
  .if %c14
  s_memrealtime s[54:55]
  .endif
  s_nop 1
  v_smfmac_f32_32x32x32_bf16 v[112:127], v[232:235], v[152:159], v236
  s_waitcnt vmcnt(0)
  .if %c14
  s_waitcnt lgkmcnt(0)
  v_mov_b32 v128, 0
  v_mov_b32 v130, s50
  v_mov_b32 v131, s51
  v_mov_b32 v132, s52
  v_mov_b32 v133, s53
  v_mov_b32 v134, s54
  v_mov_b32 v135, s55
  v_mov_b32 v136, s56
  v_mov_b32 v137, s57
  v_mov_b32 v138, s58
  v_mov_b32 v139, s59
  s_mov_b64 exec, 1
  global_store_dwordx4 v128, v[130:133], %13 offset:56
  global_store_dwordx2 v128, v[134:135], %13 offset:72
  global_store_dwordx2 v128, v[136:137], %13 offset:32
  global_store_dwordx2 v128, v[138:139], %13 offset:104
  s_mov_b64 exec, -1
  s_waitcnt vmcnt(0)
  .endif
  s_nop 15
.purgem LOADK
.purgem LOADV
.purgem EXPS
.purgem TILE
.purgem TRY
)ASM"
    : "=&{v[0:15]}"(o[0]), "=&{v[16:31]}"(o[1]), "=&{v[32:47]}"(o[2]), "=&{v[48:63]}"(o[3]),
      "=&{v[64:79]}"(o[4]), "=&{v[80:95]}"(o[5]), "=&{v[96:111]}"(o[6]), "=&{v[112:127]}"(o[7])
    : "s"(rsrc), "s"(flagp), "s"(tag), "s"(qlds), "s"(first), "s"(dbg), "i"(STAMPON)
    : "memory", "vcc", "scc", "s40", "s41", "s42", "s43", "s44", "s45", "s46", "s47", "s48", "s49", "s50", "s51", "s52", "s53", "s54", "s55", "s56", "s57", "s58", "s59",
      "v128", "v129", "v130", "v131", "v132", "v133", "v134", "v135", "v136", "v137", "v138", "v139", "v140", "v141", "v142", "v143",
      "v144", "v145", "v146", "v147", "v148", "v149", "v150", "v151", "v152", "v153", "v154", "v155", "v156", "v157", "v158", "v159",
      "v160", "v161", "v162", "v163", "v164", "v165", "v166", "v167", "v168", "v169", "v170", "v171", "v172", "v173", "v174", "v175",
      "v176", "v177", "v178", "v179", "v180", "v181", "v182", "v183", "v184", "v185", "v186", "v187", "v188", "v189", "v190", "v191",
      "v192", "v193", "v194", "v195", "v196", "v197", "v198", "v199", "v200", "v201", "v202", "v203", "v204", "v205", "v206", "v207",
      "v208", "v209", "v210", "v211", "v212", "v213", "v214", "v215", "v216", "v217", "v218", "v219", "v220", "v221", "v222", "v223",
      "v224", "v225", "v226", "v227", "v228", "v229", "v230", "v231", "v232", "v233", "v234", "v235", "v236", "v237", "v238", "v239");
}

typedef short s16x4v __attribute__((ext_vector_type(4)));

#define PAD1 7
#define PAD2 3
__device__ __forceinline__ void ffn1_core(f32x16 (&acc)[4], u32x4 rsrc, unsigned ring, unsigned vt0, unsigned vt1, unsigned vx, unsigned voff) {
  asm volatile(R"ASM(
.macro GAP nf
  .if %c10 > \nf
  s_nop (%c10-\nf-1)
  .endif
.endm
.macro DMA1 st
  s_add_u32 m0, %5, (\st&7)*2048
  s_add_u32 s41, s40, 26624
  buffer_load_dwordx4 %9, %4, s40 offen lds
  buffer_load_dwordx4 %9, %4, s41 offen offset:1024 lds
  s_add_u32 s40, s40, 55296
.endm
.macro RDA0 st
  ds_read_b64_tr_b16 v[64+16*(\st&1):65+16*(\st&1)], %6 offset:(\st&7)*2048
  ds_read_b64_tr_b16 v[66+16*(\st&1):67+16*(\st&1)], %6 offset:(\st&7)*2048+1024
.endm
.macro RDA1 st
  ds_read_b64_tr_b16 v[68+16*(\st&1):69+16*(\st&1)], %7 offset:(\st&7)*2048
  ds_read_b64_tr_b16 v[70+16*(\st&1):71+16*(\st&1)], %7 offset:(\st&7)*2048+1024
.endm
.macro RDB st
  ds_read_b128 v[72+16*(\st&1):75+16*(\st&1)], %8 offset:\st*32
  ds_read_b128 v[76+16*(\st&1):79+16*(\st&1)], %8 offset:\st*32+41472
.endm
.macro MM d, a, b, st
  .if \st == 0
  v_mfma_f32_32x32x16_bf16 v[\d:\d+15], v[\a:\a+3], v[\b:\b+3], 0
  .else
  v_mfma_f32_32x32x16_bf16 v[\d:\d+15], v[\a:\a+3], v[\b:\b+3], v[\d:\d+15]
  .endif
.endm
.macro STEP st
  .set pb, 64+16*(\st&1)
  .if \st <= 32
  s_waitcnt vmcnt(12) lgkmcnt(2)
  .elseif \st < 39
  s_waitcnt vmcnt(2*(38-\st)) lgkmcnt(2)
  .else
  s_waitcnt lgkmcnt(2)
  .endif
  MM 0, pb, (pb+8), \st
  .if \st < 39
  RDA0 (\st+1)
  .endif
  GAP 2
  MM 16, pb, (pb+12), \st
  .if \st < 39
  RDB (\st+1)
  s_waitcnt lgkmcnt(4)
  .else
  s_waitcnt lgkmcnt(0)
  .endif
  GAP 3
  MM 32, (pb+4), (pb+8), \st
  .if \st < 39
  RDA1 (\st+1)
  .endif
  GAP 2
  MM 48, (pb+4), (pb+12), \st
  .if \st < 32
  DMA1 \st
  GAP 6
  .else
  GAP 1
  .endif
.endm
  s_mov_b32 s42, m0
  s_mov_b32 s40, 0
  .set si, 0
  .rept 8
  DMA1 si
  .set si, si+1
  .endr
  s_waitcnt vmcnt(14)
  RDA0 0
  RDB 0
  RDA1 0
  .set si, 0
  .rept 40
  STEP si
  .set si, si+1
  .endr
  s_mov_b32 m0, s42
  s_nop 15
.purgem GAP
.purgem DMA1
.purgem RDA0
.purgem RDA1
.purgem RDB
.purgem MM
.purgem STEP
)ASM"
    : "=&{v[0:15]}"(acc[0]), "=&{v[16:31]}"(acc[1]), "=&{v[32:47]}"(acc[2]), "=&{v[48:63]}"(acc[3])
    : "s"(rsrc), "s"(ring), "v"(vt0), "v"(vt1), "v"(vx), "v"(voff), "i"(PAD1)
    : "memory", "scc", "s40", "s41", "s42",
      "v64", "v65", "v66", "v67", "v68", "v69", "v70", "v71", "v72", "v73", "v74", "v75", "v76", "v77", "v78", "v79",
      "v80", "v81", "v82", "v83", "v84", "v85", "v86", "v87", "v88", "v89", "v90", "v91", "v92", "v93", "v94", "v95");
}

__device__ __forceinline__ void ffn2_core(f32x16 (&acc)[5], u32x4 rw, u32x4 rh, unsigned ring, unsigned hring, unsigned ns, unsigned vwr, unsigned vhr,
                                          unsigned w0, unsigned w1, unsigned w2, unsigned w3, unsigned w4, unsigned vh) {
  asm volatile(R"ASM(
.macro GAP nf
  .if %c18 > \nf
  s_nop (%c18-\nf-1)
  .endif
.endm
.macro RDW n, hi
  .if (\n) < 70
  ds_read_b64_tr_b16 v[112+4*((\n)%%3)+2*\hi:113+4*((\n)%%3)+2*\hi], %10 offset:(((\n)/5)%%3)*5120+((\n)%%5)*32+1280*\hi
  .endif
.endm
.macro RDH i, t
  .if (\i) < 14
  ds_read_b128 v[80+16*((\i)&1)+4*\t:83+16*((\i)&1)+4*\t], %11 offset:((\i)%%3)*4096+1024*\t
  .endif
.endm
.macro MM i, c, t
  .set md, 16*\c+4*\t
  .set ma, 112+4*((5*\i+\c)%%3)
  .set mb, 80+16*(\i&1)+4*\t
  .if \i == 0
  v_mfma_f32_16x16x32_bf16 v[md:md+3], v[ma:ma+3], v[mb:mb+3], 0
  .else
  v_mfma_f32_16x16x32_bf16 v[md:md+3], v[ma:ma+3], v[mb:mb+3], v[md:md+3]
  .endif
.endm
.macro STEP i
  .set sl, (\i+2)%%3
  s_waitcnt lgkmcnt(2)
  MM \i, 0, 0
  RDW (5*\i+2), 0
  GAP 1
  MM \i, 0, 1
  RDW (5*\i+2), 1
  s_waitcnt lgkmcnt(2)
  GAP 2
  MM \i, 0, 2
  s_add_u32 m0, %7, sl*5120
  s_nop 0
  buffer_load_dwordx4 %12, %5, s40 offen lds
  GAP 3
  MM \i, 0, 3
  buffer_load_dwordx4 %13, %5, s40 offen offset:1024 lds
  buffer_load_dwordx4 %14, %5, s40 offen offset:2048 lds
  GAP 2
  MM \i, 1, 0
  RDW (5*\i+3), 0
  GAP 1
  MM \i, 1, 1
  RDW (5*\i+3), 1
  GAP 1
  MM \i, 1, 2
  buffer_load_dwordx4 %15, %5, s40 offen offset:3072 lds
  s_add_u32 m0, %7, sl*5120+4096
  GAP 2
  MM \i, 1, 3
  buffer_load_dwordx4 %16, %5, s40 offen lds
  s_add_u32 m0, %8, sl*4096
  s_waitcnt lgkmcnt(2)
  GAP 3
  MM \i, 2, 0
  RDW (5*\i+4), 0
  GAP 1
  MM \i, 2, 1
  RDW (5*\i+4), 1
  GAP 1
  MM \i, 2, 2
  buffer_load_dwordx4 %17, %6, s41 offen sc1 lds
  buffer_load_dwordx4 %17, %6, s41 offen offset:1024 sc1 lds
  GAP 2
  MM \i, 2, 3
  buffer_load_dwordx4 %17, %6, s41 offen offset:2048 sc1 lds
  buffer_load_dwordx4 %17, %6, s41 offen offset:3072 sc1 lds
  s_waitcnt vmcnt(9) lgkmcnt(2)
  GAP 3
  MM \i, 3, 0
  RDW (5*\i+5), 0
  s_add_u32 s40, s40, 40960
  GAP 2
  MM \i, 3, 1
  RDW (5*\i+5), 1
  s_add_u32 s41, s41, 4096
  GAP 2
  MM \i, 3, 2
  RDH (\i+1), 0
  GAP 1
  MM \i, 3, 3
  RDH (\i+1), 1
  .if \i < 13
  s_waitcnt lgkmcnt(4)
  .else
  s_waitcnt lgkmcnt(0)
  .endif
  GAP 2
  MM \i, 4, 0
  RDW (5*\i+6), 0
  GAP 1
  MM \i, 4, 1
  RDW (5*\i+6), 1
  GAP 1
  MM \i, 4, 2
  RDH (\i+1), 2
  GAP 1
  MM \i, 4, 3
  RDH (\i+1), 3
  GAP 1
.endm
  s_mov_b32 s42, m0
  s_mov_b32 s40, 81920
  s_mov_b32 s41, 8192
  s_waitcnt vmcnt(4)
  RDW 0, 0
  RDW 0, 1
  RDW 1, 0
  RDW 1, 1
  RDH 0, 0
  RDH 0, 1
  RDH 0, 2
  RDH 0, 3
  .set si, 0
  .rept 13
  STEP si
  .set si, si+1
  .endr
  s_cmp_eq_u32 %9, 13
  s_cbranch_scc1 Lend%=
  STEP 13
Lend%=:
  s_waitcnt vmcnt(0) lgkmcnt(0)
  s_mov_b32 m0, s42
  s_nop 7
.purgem GAP
.purgem RDW
.purgem RDH
.purgem MM
.purgem STEP
)ASM"
    : "=&{v[0:15]}"(acc[0]), "=&{v[16:31]}"(acc[1]), "=&{v[32:47]}"(acc[2]), "=&{v[48:63]}"(acc[3]), "=&{v[64:79]}"(acc[4])
    : "s"(rw), "s"(rh), "s"(ring), "s"(hring), "s"(ns), "v"(vwr), "v"(vhr), "v"(w0), "v"(w1), "v"(w2), "v"(w3), "v"(w4), "v"(vh), "i"(PAD2)
    : "memory", "scc", "s40", "s41", "s42",
      "v80", "v81", "v82", "v83", "v84", "v85", "v86", "v87", "v88", "v89", "v90", "v91", "v92", "v93", "v94", "v95",
      "v96", "v97", "v98", "v99", "v100", "v101", "v102", "v103", "v104", "v105", "v106", "v107", "v108", "v109", "v110", "v111",
      "v112", "v113", "v114", "v115", "v116", "v117", "v118", "v119", "v120", "v121", "v122", "v123");
}
__device__ __forceinline__ u32x4 mk_rsrc(const void* p, unsigned bytes) {
  return (u32x4){rfl((unsigned)(u64)p), rfl((unsigned)((u64)p >> 32)), bytes, 0x00020000u};
}

__global__ __launch_bounds__(512, 2) void fused_kernel(const u16* __restrict__ x, const u16* __restrict__ wq, const u16* __restrict__ wk,
                                                       const u16* __restrict__ wv, const u16* __restrict__ wo, const u16* __restrict__ w1,
                                                       const u16* __restrict__ w2, u16* __restrict__ out, char* __restrict__ ws) {
  extern __shared__ __attribute__((aligned(16))) char smem[];
  const unsigned lb = (unsigned)(uintptr_t)smem;
  const int b = blockIdx.x, xcd = b & 7, idx = b >> 3;
  const int rg = (xcd >> 1) * 8 + (idx >> 2), j = (xcd & 1) * 4 + (idx & 3);
  const int lane = threadIdx.x & 63, w = rfl(threadIdx.x >> 6);
  const int i16 = lane & 15, g4 = lane >> 4, tq = i16 >> 2, tp = lane & 3;
  STAMPT(0);

  u32x4 xv[10];
  u64 ep;
  {
    const char* xb_ = (const char*)x + (size_t)(rg * 64 + w * 8) * 1280;
    const unsigned xo = (lane >> 3) * 1280 + (lane & 7) * 16;
    asm volatile("global_load_dwordx4 %0, %11, %12\n\tglobal_load_dwordx4 %1, %11, %12 offset:128\n\tglobal_load_dwordx4 %2, %11, %12 offset:256\n\t"
                 "global_load_dwordx4 %3, %11, %12 offset:384\n\tglobal_load_dwordx4 %4, %11, %12 offset:512\n\tglobal_load_dwordx4 %5, %11, %12 offset:640\n\t"
                 "global_load_dwordx4 %6, %11, %12 offset:768\n\tglobal_load_dwordx4 %7, %11, %12 offset:896\n\tglobal_load_dwordx4 %8, %11, %12 offset:1024\n\t"
                 "global_load_dwordx4 %9, %11, %12 offset:1152\n\tglobal_load_dwordx2 %10, %13, %14 sc1"
                 : "=&v"(xv[0]), "=&v"(xv[1]), "=&v"(xv[2]), "=&v"(xv[3]), "=&v"(xv[4]), "=&v"(xv[5]), "=&v"(xv[6]), "=&v"(xv[7]), "=&v"(xv[8]), "=&v"(xv[9]), "=&v"(ep)
                 : "v"(xo), "s"(xb_), "v"(0u), "s"(ws + W_EPOCH) : "memory");
  }
  const char* wqb = (const char*)wq + j * 160;
  {
    const char* wkv = (const char*)((j & 1) ? wv : wk);
#pragma unroll
    for (int u = 0; u < 8; ++u) {
      const int p = w + 8 * u;
      const unsigned c = 64 * (p - 25) + lane;
      const bool isw = p < 25;
      dma16(isw ? wkv + p * 1024 : wqb, isw ? lane * 16 : (c / 10) * 1280 + (c % 10) * 16, lb + L_R + p * 1024);
    }
    if (w == 0) { const unsigned c = 64 * 39 + lane; dma16(wqb, (c / 10) * 1280 + (c % 10) * 16, lb + L_R + 64 * 1024); }
  }
  asm volatile("s_waitcnt vmcnt(14)" : "+v"(xv[0]), "+v"(xv[1]), "+v"(xv[2]), "+v"(xv[3]), "+v"(xv[4]) :: "memory");

  {
    float s1 = 0.f, s2 = 0.f, s1b = 0.f, s2b = 0.f;
    const bfx2 one = __builtin_bit_cast(bfx2, 0x3f803f80u);
#pragma unroll
    for (int i = 0; i < 10; ++i) {
      if (i == 5) asm volatile("s_waitcnt vmcnt(8)" : "+v"(xv[5]), "+v"(xv[6]), "+v"(xv[7]), "+v"(xv[8]), "+v"(xv[9]), "+v"(ep), "+v"(s1), "+v"(s2), "+v"(s1b), "+v"(s2b) :: "memory");
#pragma unroll
      for (int e = 0; e < 4; e += 2) {
        const unsigned u0 = xv[i][e], u1 = xv[i][e + 1];
        bfx2 p0 = __builtin_bit_cast(bfx2, u0), p1 = __builtin_bit_cast(bfx2, u1);
        s1 = __builtin_amdgcn_fdot2_f32_bf16(p0, one, s1, false);  s2 = __builtin_amdgcn_fdot2_f32_bf16(p0, p0, s2, false);
        s1b = __builtin_amdgcn_fdot2_f32_bf16(p1, one, s1b, false); s2b = __builtin_amdgcn_fdot2_f32_bf16(p1, p1, s2b, false);
      }
    }
    s1 += s1b; s2 += s2b;
    s1 += __builtin_amdgcn_update_dpp(0.f, s1, 0xB1, 0xf, 0xf, true);  s2 += __builtin_amdgcn_update_dpp(0.f, s2, 0xB1, 0xf, 0xf, true);
    s1 += __builtin_amdgcn_update_dpp(0.f, s1, 0x4E, 0xf, 0xf, true);  s2 += __builtin_amdgcn_update_dpp(0.f, s2, 0x4E, 0xf, 0xf, true);
    s1 += __builtin_amdgcn_update_dpp(0.f, s1, 0x141, 0xf, 0xf, true); s2 += __builtin_amdgcn_update_dpp(0.f, s2, 0x141, 0xf, 0xf, true);
    const float mean = s1 * (1.f / D);
    const float var = fmaxf(s2 * (1.f / D) - mean * mean, 0.f);
    const float m = rbf(mean), sd = rbf(__builtin_sqrtf(rbf(rbf(var) + 1e-5f)));
    const float r = 1.f / sd, mr = -m * r;
    const unsigned xa = lb + L_XN + (w * 8 + (lane >> 3)) * XP + (lane & 4) * 16 + (lane & 2) * 16 + (lane & 1) * 8;
#pragma unroll
    for (int i = 0; i < 10; ++i) {
      u32x4 y;
#pragma unroll
      for (int e = 0; e < 4; ++e) {
        float lo = __uint_as_float(xv[i][e] << 16), hi = __uint_as_float(xv[i][e] & 0xffff0000u);
        y[e] = pk(__builtin_fmaf(lo, r, mr), __builtin_fmaf(hi, r, mr));
      }
      *(LAS u32x2*)(uintptr_t)(xa + i * 128) = (u32x2){y[0], y[1]};
      *(LAS u32x2*)(uintptr_t)(xa + i * 128 + 16) = (u32x2){y[2], y[3]};
    }
    if (threadIdx.x < 256) *(LAS u32x2*)(uintptr_t)(lb + L_QI + threadIdx.x * 48 + 40) = (u32x2){0u, 0u};
    if (threadIdx.x == 256) *(LAS unsigned*)(uintptr_t)(lb + L_MISC) = 0u;
  }
  const u64 tag = (((u64)rfl((unsigned)(ep >> 32)) << 32) | rfl((unsigned)ep)) + 1;
  STAMPT(1);
  VMWAIT(0);
  BARRIER();
  STAMPT(2);

  const unsigned xf16 = lb + L_XN + i16 * XP + g4 * 16;
  const int f0 = 16 * (g4 >> 1) + 4 * (g4 & 1) + tq;
  const unsigned wr = f0 * 160 + tp * 8;

  if (w < 4) {
    const int t16 = rg * 4 + (j >> 1);
    if (w < 2) {
      f32x4 acc = {0.f, 0.f, 0.f, 0.f};
      const unsigned wa = lb + L_R + f0 * 40 + w * 32 + tp * 8, xa = xf16 + (j >> 1) * 16 * XP;
      if (j & 1) {
#pragma unroll
        for (int s = 0; s < 20; ++s) acc = MFMA16(ld128(xa + s * 64), ldtr(wa + s * 1280, wa + s * 1280 + 320), acc);
      } else {
#pragma unroll
        for (int s = 0; s < 20; ++s) acc = MFMA16(ldtr(wa + s * 1280, wa + s * 1280 + 320), ld128(xa + s * 64), acc);
      }
      char* kv = ws + W_KV + (t16 >> 1) * 2560;
      const u64 val = (u64)pk(acc[0], acc[1]) | ((u64)pk(acc[2], acc[3]) << 32);
      if (j & 1) {
        if (w == 0 || i16 < 4) st_sc1_64(kv + 1280 + (((t16 & 1) * 2 + (g4 >> 1)) * 20 + 16 * w + i16) * 16 + (g4 & 1) * 8, val);
      } else {
        const int key = (t16 & 1) * 16 + i16;
        if (w == 0) st_sc1_64(kv + ((g4 >> 1) * 32 + key) * 16 + (g4 & 1) * 8, val);
        else if (g4 == 0) st_sc1_64(kv + 1024 + key * 8, val);
      }
    }
    STAMPT(3);
    BARRIER();
    if (w < 2) {
      VMWAIT(0);
      if (lane == 0) st_sc1_64(ws + W_F1 + ((t16 * 2 + (j & 1)) * 2 + w) * 8, tag);
    }
    const char* kvq = ws + W_KV + w * 40960;
    const u32x4 rsrc = mk_rsrc(kvq, 40960u);
    const char* flagp = rflp(ws + W_F1 + w * 1024);
    const u64 tg = tag;
#pragma nounroll
    for (int attempt = 0;; ++attempt) {
      f32x16 o[8];
      attn_core(o, rsrc, flagp, tg, rfl(lb + L_QI), rfl(attempt == 0 ? 1u : 0u), (u64*)(ws + W_STAMP) + (blockIdx.x * 8 + w) * 16);
      const int lane = fresh_lane(), r32 = lane & 31, h = lane >> 5;
      STAMPT(5); STAMPC(14);
      BARRIER();
#pragma unroll
      for (int qt = 0; qt < 8; ++qt) {
        const int own = qt >> 1;
        if (own != w) {
          const int slot = (w - own - 1) & 3;
          const unsigned a = lb + L_XN + (qt * 3 + slot) * 3072 + lane * 16;
#pragma unroll
          for (int qd = 0; qd < 3; ++qd) st128(a + qd * 1024, (f32x4){o[qt][4 * qd], o[qt][4 * qd + 1], o[qt][4 * qd + 2], o[qt][4 * qd + 3]});
        }
      }
      f32x4 t[2][3];
#pragma unroll
      for (int ww = 0; ww < 4; ++ww) if (ww == 0 || ww == w) {
#pragma unroll
        for (int u = 0; u < 2; ++u)
#pragma unroll
          for (int qd = 0; qd < 3; ++qd) t[u][qd] = (f32x4){o[2 * ww + u][4 * qd], o[2 * ww + u][4 * qd + 1], o[2 * ww + u][4 * qd + 2], o[2 * ww + u][4 * qd + 3]};
      }
      BARRIER();
      bool bad = false;
#pragma unroll
      for (int u = 0; u < 2; ++u) {
        f32x4 acc[3];
#pragma unroll
        for (int qd = 0; qd < 3; ++qd) {
          const unsigned a = lb + L_XN + ((2 * w + u) * 3) * 3072 + qd * 1024 + lane * 16;
          f32x4 p0 = *(const LAS f32x4*)(uintptr_t)a, p1 = *(const LAS f32x4*)(uintptr_t)(a + 3072), p2 = *(const LAS f32x4*)(uintptr_t)(a + 6144);
          acc[qd] = (t[u][qd] + p0) + (p1 + p2);
        }
        auto sw = __builtin_amdgcn_permlane32_swap(__float_as_uint(acc[2][0]), __float_as_uint(acc[2][0]), false, false);
        const float l = __uint_as_float(sw[1]);
        bad |= !(l >= 0x1p-64f && l <= 0x1p100f);
        const float rl = 1.f / l;
        const int rt16 = 2 * u + (r32 >> 4);
        char* ob = ws + W_O + (size_t)rg * 81920 + rt16 * 1024 + (r32 & 15) * 16;
        const int gc = 10 * j + 2 * w + h;
        const u32x4 v = {pk(acc[0][0] * rl, acc[0][1] * rl), pk(acc[0][2] * rl, acc[0][3] * rl), pk(acc[1][0] * rl, acc[1][1] * rl), pk(acc[1][2] * rl, acc[1][3] * rl)};
        char* p = ob + (gc >> 2) * 4096 + (gc & 3) * 256;
        asm volatile("global_store_dwordx4 %0, %1, off sc1\n\ts_nop 1" :: "v"(p), "v"(v) : "memory");
        const int gl = 10 * j + 8 + (w & 1);
        if (h == 0) st_sc1_64(ob + (gl >> 2) * 4096 + (gl & 3) * 256 + (w >> 1) * 8,
                              (u64)pk(acc[2][0] * rl, acc[2][1] * rl) | ((u64)pk(acc[2][2] * rl, acc[2][3] * rl) << 32));
      }
      if (attempt == 0 && __any(bad) && lane == 0) *(volatile LAS unsigned*)(uintptr_t)(lb + L_MISC) = 1u;
      VMWAIT(0);
      BARRIER();
      const unsigned redo = attempt == 0 ? rfl(*(volatile LAS unsigned*)(uintptr_t)(lb + L_MISC)) : 0u;
      if (!redo) break;
      {
        const char* kq = kvq;
        for (int qt = 0; qt < 8; ++qt) {
          const unsigned qa_ = lb + L_QI + qt * 1536 + r32 * 48;
          const bfx8 q1 = ld128(qa_ + h * 16);
          u32x2 q2u = *(const LAS u32x2*)(uintptr_t)(qa_ + 32);
          if (h) q2u = (u32x2){0u, 0u};
          float mx = -__builtin_inff();
          for (int kt = 0; kt < 16; ++kt) {
            const char* tp_ = kq + kt * 2560;
            u32x4 k1 = {0u, 0u, 0u, 0u};
            u64 a0 = ld_sc1_64(tp_ + lane * 16), a1 = ld_sc1_64(tp_ + lane * 16 + 8), a2 = ld_sc1_64(tp_ + 1024 + r32 * 8);
            k1 = (u32x4){(unsigned)a0, (unsigned)(a0 >> 32), (unsigned)a1, (unsigned)(a1 >> 32)};
            u32x2 k2 = {(unsigned)a2, (unsigned)(a2 >> 32)};
            f32x16 sc = {};
            sc = __builtin_amdgcn_mfma_f32_32x32x8bf16_1k(__builtin_bit_cast(s16x4v, k2), __builtin_bit_cast(s16x4v, q2u), sc, 0, 0, 0);
            sc = MFMA32(__builtin_bit_cast(bfx8, k1), q1, sc);
#pragma unroll
            for (int e = 0; e < 16; ++e) mx = fmaxf(mx, sc[e]);
          }
          auto s2 = __builtin_amdgcn_permlane32_swap(__float_as_uint(mx), __float_as_uint(mx), false, false);
          mx = fmaxf(__uint_as_float(s2[0]), __uint_as_float(s2[1]));
          if (h == 0) *(LAS float*)(uintptr_t)(lb + L_XN + ((w * 8 + qt) * 32 + r32) * 4) = mx;
        }
      }
      BARRIER();
      {
        const int q = w * 64 + lane;
        float mx = -__builtin_inff();
#pragma unroll
        for (int ww = 0; ww < 4; ++ww) mx = fmaxf(mx, *(const LAS float*)(uintptr_t)(lb + L_XN + (ww * 256 + q) * 4));
        const float m0 = rbf(mx), m1 = rbf(mx - m0), m2 = rbf(mx - m0 - m1);
        *(LAS u32x2*)(uintptr_t)(lb + L_QI + q * 48 + 40) = (u32x2){pk(-m0, -m1), pk(-m2, 0.f)};
      }
      BARRIER();
    }
    const int lane = fresh_lane();
    if (w == 0 && lane == 0) st_sc1_64(ws + W_F3 + rg * 128 + j * 8, tg);
    if (b == 0 && w == 1 && lane == 0) st_sc1_64(ws + W_EPOCH, tg);
    STAMPT(6);
    BARRIER();
  } else {
    const int gw = w - 4;
    const unsigned ring = lb + L_R + gw * 16384;
    const int r32 = lane & 31, h = lane >> 5;
    unsigned woff[5];
#pragma unroll
    for (int m = 0; m < 5; ++m) { const unsigned c = 64 * m + lane; woff[m] = (c / 10) * 1280 + (c % 10) * 16; }
    {
      f32x4 qa[5];
#pragma unroll
      for (int t = 0; t < 5; ++t) qa[t] = (f32x4){0.f, 0.f, 0.f, 0.f};
      auto q_steps = [&](int chunk, unsigned cb) {
#pragma unroll
        for (int s = 0; s < 4; ++s) {
          const bfx8 xf = ld128(xf16 + gw * 16 * XP + (chunk * 4 + s) * 64);
#pragma unroll
          for (int t = 0; t < 5; ++t) { const unsigned a = cb + wr + s * 5120 + t * 32; qa[t] = MFMA16(ldtr(a, a + 1280), xf, qa[t]); }
        }
      };
      q_steps(0, lb + L_C0);
      q_steps(1, lb + L_C1);
      STAMPT(3);
      BARRIER();
#pragma unroll
      for (int ch = 2; ch < 5; ++ch)
#pragma unroll
        for (int m = 0; m < 5; ++m)
          dma16(wqb + (size_t)(ch * 128 + gw * 32) * 1280, woff[m], lb + (ch == 2 ? L_R : ch == 3 ? L_C0 : L_C1) + (gw * 5 + m) * 1024);
      VMWAIT(10); BARRIER();
      q_steps(2, lb + L_R);
      VMWAIT(5); BARRIER();
      q_steps(3, lb + L_C0);
      VMWAIT(0); BARRIER();
      q_steps(4, lb + L_C1);
#pragma unroll
      for (int t = 0; t < 5; ++t) {
        const int f = 16 * t + 4 * g4, head = f / 20, d = f % 20;
        *(LAS u32x2*)(uintptr_t)(lb + L_QI + (head * 64 + gw * 16 + i16) * 48 + d * 2) =
            (u32x2){pk(qa[t][0] * QSCALE, qa[t][1] * QSCALE), pk(qa[t][2] * QSCALE, qa[t][3] * QSCALE)};
      }
      BARRIER();
    }
    STAMPT(4); STAMPC(13);
    __builtin_amdgcn_s_setprio(1);
    const int nun = j < 3 ? 4 : 3, unit = (j < 3 ? 4 * j : 12 + 3 * (j - 3)) + gw;
    char* hbase = ws + W_H + (size_t)rg * 54 * 4096;
    const unsigned hring = lb + L_XN + gw * 20480;
    const char* wob = (const char*)wo + (size_t)gw * 160 * 1280 + j * 160;
    unsigned to[25];
#pragma unroll
    for (int i = 0; i < 25; ++i) to[i] = wotab.v[i * 64 + lane];
    if (gw < nun) {
      const unsigned voff = (lane >> 3) * 3456 + (((lane & 7) ^ (((lane >> 4) & 1) << 2)) * 16);
      const unsigned tb = ring + (4 * (g4 >> 1) + tq) * 128 + (tp & 1) * 8;
      const unsigned tr0 = tb + ((2 * (g4 & 1) + (tp >> 1)) + 4 * (tq >> 1)) * 16, tr1 = tb + ((2 * (g4 & 1) + (tp >> 1)) + 4 * (1 - (tq >> 1))) * 16;
      f32x16 acc[4];
      ffn1_core(acc, mk_rsrc((const char*)w1 + unit * 128, 640 * 3456), rfl(ring), tr0, tr1, lb + L_XN + r32 * XP + h * 16, voff);
#pragma unroll
      for (int i = 0; i < 25; ++i) asm volatile("" : "+v"(to[i]));
#pragma unroll
      for (int nt = 0; nt < 2; ++nt)
#pragma unroll
        for (int rt = 0; rt < 2; ++rt)
#pragma unroll
          for (int c = 0; c < 2; ++c) {
            u32x4 v;
#pragma unroll
            for (int e = 0; e < 4; ++e)
              v[e] = __builtin_bit_cast(unsigned, __builtin_elementwise_max(__builtin_bit_cast(s16x2, pk(acc[2 * nt + rt][8 * c + 2 * e], acc[2 * nt + rt][8 * c + 2 * e + 1])), (s16x2){0, 0}));
            char* p = hbase + (((2 * unit + nt) * 4 + 2 * rt + (r32 >> 4)) * 4 + 2 * c + h) * 256 + (r32 & 15) * 16;
            asm volatile("global_store_dwordx4 %0, %1, off sc1\n\ts_nop 1" :: "v"(p), "v"(v) : "memory");
          }
    }
    STAMPT(5);
    bfx8 wof[5][5];
#pragma unroll
    for (int s = 0; s < 3; ++s)
#pragma unroll
      for (int m = 0; m < 5; ++m) dma16(wob, to[s * 5 + m], ring + s * 5120 + m * 1024);
    VMWAIT(15);
    if (gw < nun && lane == 0) st_sc1_64(ws + W_F2 + rg * 256 + unit * 8, tag);
    VMWAIT(0);
#pragma unroll
    for (int s = 0; s < 3; ++s)
#pragma unroll
      for (int c = 0; c < 5; ++c) { const unsigned a = ring + s * 5120 + wr + c * 32; wof[s][c] = ldtr(a, a + 1280); }
    VMWAIT(0);
#pragma unroll
    for (int s = 3; s < 5; ++s)
#pragma unroll
      for (int m = 0; m < 5; ++m) dma16(wob, to[s * 5 + m], ring + (s - 3) * 5120 + m * 1024);
    VMWAIT(0);
#pragma unroll
    for (int s = 3; s < 5; ++s)
#pragma unroll
      for (int c = 0; c < 5; ++c) { const unsigned a = ring + (s - 3) * 5120 + wr + c * 32; wof[s][c] = ldtr(a, a + 1280); }
    VMWAIT(0);
    STAMPT(7);
    const int s0 = gw < 2 ? 14 * gw : 28 + 13 * (gw - 2), ns = gw < 2 ? 14 : 13;
    const char* w2b = (const char*)w2 + j * 160 + (size_t)s0 * 32 * 1280;
    const char* hb = hbase + s0 * 4096;
#pragma unroll
    for (int i = 0; i < 2; ++i)
#pragma unroll
      for (int m = 0; m < 5; ++m) dma16(w2b + (size_t)i * 32 * 1280, woff[m], ring + i * 5120 + m * 1024);
    poll(ws + W_F2 + rg * 256 + (lane < 27 ? lane : 0) * 8, lane < 27, tag);
    VMWAIT(0);
    STAMPT(6);
#pragma unroll
    for (int i = 0; i < 2; ++i)
#pragma unroll
      for (int m = 0; m < 4; ++m) dma16_sc1(hb + i * 4096 + m * 1024, lane * 16, hring + i * 4096 + m * 1024);
    f32x16 a5[5];
    ffn2_core(a5, mk_rsrc(w2b, ns * 40960), mk_rsrc(hb, ns * 4096), rfl(ring), rfl(hring), rfl(ns), ring + wr, hring + lane * 16,
              woff[0], woff[1] - 1024, woff[2] - 2048, woff[3] - 3072, woff[4], lane * 16);
    f32x4 acc[5][4];
#pragma unroll
    for (int c = 0; c < 5; ++c)
#pragma unroll
      for (int t = 0; t < 4; ++t) acc[c][t] = (f32x4){a5[c][4 * t], a5[c][4 * t + 1], a5[c][4 * t + 2], a5[c][4 * t + 3]};
    u32x2 xres[5];
    const size_t orow = (size_t)(rg * 64 + gw * 16 + i16) * D + j * 80 + g4 * 4;
#pragma unroll
    for (int c = 0; c < 5; ++c) xres[c] = *(const u32x2*)(x + orow + c * 16);
    STAMPT(8); STAMPC(14);
    BARRIER();
    BARRIER();
    BARRIER();
    if (rfl(*(volatile LAS unsigned*)(uintptr_t)(lb + L_MISC))) { BARRIER(); BARRIER(); BARRIER(); BARRIER(); BARRIER(); }
    STAMPT(9);
    poll(ws + W_F3 + rg * 128 + (2 * gw + (lane & 1)) * 8, true, tag);
    STAMPT(10);
    {
      const auto orsrc = __builtin_amdgcn_make_buffer_rsrc((void*)rflp(ws + W_O + (size_t)rg * 81920 + gw * 20480), 0, 20480, 0x00020000);
      u32x4 of[5][4];
#pragma unroll
      for (int s = 0; s < 5; ++s)
#pragma unroll
        for (int t = 0; t < 4; ++t) of[s][t] = __builtin_amdgcn_raw_buffer_load_b128(orsrc, lane * 16 + (s * 4 + t) * 1024, 0, 16);
#pragma unroll
      for (int s = 0; s < 5; ++s)
#pragma unroll
        for (int c = 0; c < 5; ++c)
#pragma unroll
          for (int t = 0; t < 4; ++t) acc[c][t] = MFMA16(wof[s][c], __builtin_bit_cast(bfx8, of[s][t]), acc[c][t]);
    }
    STAMPT(11);
#pragma unroll
    for (int t = 0; t < 4; ++t) if (t != gw) {
      const int sl = (gw - t - 1) & 3;
#pragma unroll
      for (int c = 0; c < 5; ++c) st128(lb + L_R + ((t * 3 + sl) * 5 + c) * 1024 + lane * 16, acc[c][t]);
    }
    f32x4 mine[5];
#pragma unroll
    for (int t = 0; t < 4; ++t) if (t == 0 || t == gw) {
#pragma unroll
      for (int c = 0; c < 5; ++c) mine[c] = acc[c][t];
    }
    BARRIER();
#pragma unroll
    for (int c = 0; c < 5; ++c) {
      const unsigned a = lb + L_R + (gw * 15 + c) * 1024 + lane * 16;
      f32x4 p0 = *(const LAS f32x4*)(uintptr_t)a, p1 = *(const LAS f32x4*)(uintptr_t)(a + 5120), p2 = *(const LAS f32x4*)(uintptr_t)(a + 10240);
      f32x4 v = (mine[c] + p0) + (p1 + p2);
      v[0] += __uint_as_float(xres[c][0] << 16); v[1] += __uint_as_float(xres[c][0] & 0xffff0000u);
      v[2] += __uint_as_float(xres[c][1] << 16); v[3] += __uint_as_float(xres[c][1] & 0xffff0000u);
      *(u32x2*)(out + orow + c * 16) = (u32x2){pk(v[0], v[1]), pk(v[2], v[3])};
    }
    STAMPT(12);
    if (w == 4) {
      asm volatile("s_waitcnt lgkmcnt(0)" ::: "memory");
      const unsigned a0 = *(volatile LAS unsigned*)(uintptr_t)(L_MISC + 64 + (ENC_W * 16 + ENC_J) * 4), a1 = *(volatile LAS unsigned*)(uintptr_t)(L_MISC + 64 + (ENC_W * 16 + ENC_I) * 4);
      const unsigned dl = rfl((a1 - a0) * ENC_MUL / ENC_DIV), t0 = (unsigned)__builtin_amdgcn_s_memrealtime();
      while ((unsigned)__builtin_amdgcn_s_memrealtime() - t0 < (dl < 200000u ? dl : 200000u)) __builtin_amdgcn_s_sleep(8);
    }
  }
}
}

extern "C" void kernel_launch(void* const* d_in, const int* in_sizes, int n_in, void* d_out, int out_size, void* d_ws, size_t ws_size,
                              hipStream_t stream) {
  (void)in_sizes; (void)n_in; (void)out_size; (void)ws_size;
  (void)hipFuncSetAttribute((const void*)fused_kernel, hipFuncAttributeMaxDynamicSharedMemorySize, L_TOTAL);
  fused_kernel<<<256, 512, L_TOTAL, stream>>>((const u16*)d_in[0], (const u16*)d_in[1], (const u16*)d_in[2], (const u16*)d_in[3],
                                              (const u16*)d_in[4], (const u16*)d_in[6], (const u16*)d_in[7], (u16*)d_out, (char*)d_ws);
}
```

```cpp
#include <hip/hip_runtime.h>
#include <hip/hip_bf16.h>
#include <stdint.h>

typedef unsigned short u16;
typedef unsigned long long u64;
typedef __bf16 bfx2 __attribute__((ext_vector_type(2)));
typedef __bf16 bfx8 __attribute__((ext_vector_type(8)));
typedef short s16x2 __attribute__((ext_vector_type(2)));
typedef float f32x2 __attribute__((ext_vector_type(2)));
typedef float f32x4 __attribute__((ext_vector_type(4)));
typedef float f32x16 __attribute__((ext_vector_type(16)));
typedef unsigned u32x2 __attribute__((ext_vector_type(2)));
typedef unsigned u32x4 __attribute__((ext_vector_type(4)));
typedef unsigned u32x16 __attribute__((ext_vector_type(16)));
#define LAS __attribute__((address_space(3)))
#define GAS __attribute__((address_space(1)))

namespace {
constexpr int D = 640;
constexpr int XP = 1296;
constexpr int L_XN = 0, L_QI = 64 * XP, L_R = L_QI + 12288, L_MISC = L_R + 66560, L_TOTAL = 163840;
constexpr int L_C0 = L_R + 25600;
constexpr int L_OP = L_XN;
constexpr int M_W2 = 8, M_F = 24  , M_R1 = 28  , M_P = 32  ,
              M_F1 = 40  , M_E1 = 44  , M_B1 = 64, M_B2 = 68  , M_KV = 72  ;
constexpr size_t W_EPOCH = 0, W_F1 = 4096, W_F2 = 512 << 10, W_KV = 1 << 20, W_H = 2 << 20, W_O = 10 << 20;
constexpr float QSCALE = 0.22360679774997896f * 1.4426950408889634f;
constexpr unsigned SPIN_CAP = 1u << 18;
constexpr int F1S = 64;
constexpr int POLL_AT = 16, POLL_N = 2;
constexpr int PAD1 = 9, LOW1 = 28;
constexpr unsigned CODE_LINES = 312;

struct WoTab { unsigned v[64 * 28]; };
constexpr WoTab make_wotab() {
  WoTab t{};
  for (int s = 0; s < 5; ++s) for (int m = 0; m < 5; ++m) for (int l = 0; l < 64; ++l) {
    int c = 64 * m + l, r = c / 10, pos = c % 10;
    int g = 2 * (r >> 4) + ((r >> 2) & 1), tt = (r >> 3) & 1, e = r & 3;
    int gc = 4 * s + g, blk = gc / 10, ci = gc % 10;
    int row = 80 * blk + (ci < 8 ? 20 * (ci >> 1) + 4 * (ci & 1) + 8 * tt + e : 20 * ((ci - 8) + 2 * tt) + 16 + e);
    t.v[((s * 5 + m) >> 2) * 256 + l * 4 + ((s * 5 + m) & 3)] = row * 1280 + pos * 16;
  }
  return t;
}
__device__ __attribute__((aligned(128))) const WoTab wotab = make_wotab();

__device__ __forceinline__ unsigned rfl(unsigned x) { return __builtin_amdgcn_readfirstlane(x); }
__device__ __forceinline__ int fresh_lane() {
  int l; asm volatile("v_mbcnt_lo_u32_b32 %0, -1, 0\n\tv_mbcnt_hi_u32_b32 %0, -1, %0" : "=v"(l)); return l;
}
__device__ __forceinline__ void dma16(const char* sbase, unsigned voff, unsigned lds) {
  unsigned sv;
  asm volatile("s_mov_b32 %0, m0\n\ts_mov_b32 m0, %3\n\ts_nop 0\n\tglobal_load_lds_dwordx4 %1, %2\n\ts_mov_b32 m0, %0"
               : "=&s"(sv) : "v"(voff), "s"(sbase), "s"(lds) : "memory");
}
#define VMWAIT(n) asm volatile("s_waitcnt vmcnt(" #n ") lgkmcnt(0)" ::: "memory")
__device__ __forceinline__ bfx8 ld128(unsigned a) { return *(const LAS bfx8*)(uintptr_t)a; }
__device__ __forceinline__ unsigned pk(float a, float b) { f32x2 v = {a, b}; return __builtin_bit_cast(unsigned, __builtin_convertvector(v, bfx2)); }
__device__ __forceinline__ float rbf(float a) { return __uint_as_float(pk(a, a) << 16); }
#define MFMA32(a, b, c) __builtin_amdgcn_mfma_f32_32x32x16_bf16(a, b, c, 0, 0, 0)
__device__ __forceinline__ void st_sc1_64(char* p, u64 v) { __hip_atomic_store((GAS u64*)p, v, __ATOMIC_RELAXED, __HIP_MEMORY_SCOPE_AGENT); }
__device__ __forceinline__ u64 ld_sc1_64(const char* p) { return __hip_atomic_load((GAS u64*)p, __ATOMIC_RELAXED, __HIP_MEMORY_SCOPE_AGENT); }
__device__ __forceinline__ void poll(const char* p, bool need, u64 tag) {
  for (unsigned it = 0; it < SPIN_CAP; ++it) {
    const u64 v = ld_sc1_64(p);
    if (__all(!need || v == tag)) break;
    __builtin_amdgcn_s_sleep(4);
  }
  asm volatile("" ::: "memory");
}

#define V0_239 "v0", "v1", "v2", "v3", "v4", "v5", "v6", "v7", "v8", "v9", "v10", "v11", "v12", "v13", "v14", "v15", "v16", "v17", "v18", "v19", "v20", "v21", "v22", "v23", "v24", "v25", "v26", "v27", "v28", "v29", "v30", "v31", "v32", "v33", "v34", "v35", "v36", "v37", "v38", "v39", "v40", "v41", "v42", "v43", "v44", "v45", "v46", "v47", "v48", "v49", "v50", "v51", "v52", "v53", "v54", "v55", "v56", "v57", "v58", "v59", "v60", "v61", "v62", "v63", "v64", "v65", "v66", "v67", "v68", "v69", "v70", "v71", "v72", "v73", "v74", "v75", "v76", "v77", "v78", "v79", "v80", "v81", "v82", "v83", "v84", "v85", "v86", "v87", "v88", "v89", "v90", "v91", "v92", "v93", "v94", "v95", "v96", "v97", "v98", "v99", "v100", "v101", "v102", "v103", "v104", "v105", "v106", "v107", "v108", "v109", "v110", "v111", "v112", "v113", "v114", "v115", "v116", "v117", "v118", "v119", "v120", "v121", "v122", "v123", "v124", "v125", "v126", "v127", "v128", "v129", "v130", "v131", "v132", "v133", "v134", "v135", "v136", "v137", "v138", "v139", "v140", "v141", "v142", "v143", "v144", "v145", "v146", "v147", "v148", "v149", "v150", "v151", "v152", "v153", "v154", "v155", "v156", "v157", "v158", "v159", "v160", "v161", "v162", "v163", "v164", "v165", "v166", "v167", "v168", "v169", "v170", "v171", "v172", "v173", "v174", "v175", "v176", "v177", "v178", "v179", "v180", "v181", "v182", "v183", "v184", "v185", "v186", "v187", "v188", "v189", "v190", "v191", "v192", "v193", "v194", "v195", "v196", "v197", "v198", "v199", "v200", "v201", "v202", "v203", "v204", "v205", "v206", "v207", "v208", "v209", "v210", "v211", "v212", "v213", "v214", "v215", "v216", "v217", "v218", "v219", "v220", "v221", "v222", "v223", "v224", "v225", "v226", "v227", "v228", "v229", "v230", "v231", "v232", "v233", "v234", "v235", "v236", "v237", "v238", "v239"
__device__ __forceinline__ unsigned attn_core(u32x4 rsrc, const char* flagp, u64 tag, unsigned qlds, unsigned qrot, unsigned misc, unsigned thr, u32x4 ors,
                                              u32x4 aw, u32x4 ax, u32x2 ao, unsigned lr, unsigned lw) {
  unsigned redo;
  asm volatile(R"ASM(
.macro LOADKV b
  s_setprio 3
  buffer_load_dwordx4 v[208+14*\b:211+14*\b], v237, %[rs], s40 offen sc1
  s_mov_b64 exec, s[44:45]
  buffer_load_dwordx2 v[212+14*\b:213+14*\b], v238, %[rs], s40 offen offset:1024 sc1
  s_mov_b64 exec, s[46:47]
  buffer_load_dwordx4 v[214+14*\b:217+14*\b], v239, %[rs], s40 offen offset:1280 sc1
  buffer_load_dwordx4 v[218+14*\b:221+14*\b], v239, %[rs], s40 offen offset:1920 sc1
  s_mov_b64 exec, -1
  s_setprio 0
  s_add_u32 s40, s40, 2560
.endm
.macro SPIN of, id, msk
  s_mov_b32 s42, 0x100000
  s_branch Lsc\id\()_%=
Lsw\id\()_%=:
  ds_read_b32 v206, v250 offset:\of
  s_waitcnt lgkmcnt(0)
  s_sub_u32 s42, s42, 1
  s_cbranch_scc1 Lsx\id\()_%=
Lsc\id\()_%=:
  v_readfirstlane_b32 s43, v206
  s_nop 3
  .if \msk
  s_and_b32 s48, s43, 0xffff
  s_cmp_ge_u32 s48, s56
  .else
  s_cmp_ge_u32 s43, s56
  .endif
  s_cbranch_scc0 Lsw\id\()_%=
Lsx\id\()_%=:
.endm
.macro PACK i
  .set pt, 160+6*(\i&3)
  .set po, 16*\i
  v_cvt_pk_bf16_f32 v[pt], v[po], v[po+1]
  v_cvt_pk_bf16_f32 v[pt+1], v[po+2], v[po+3]
  v_cvt_pk_bf16_f32 v[pt+2], v[po+4], v[po+5]
  v_cvt_pk_bf16_f32 v[pt+3], v[po+6], v[po+7]
  v_cvt_pk_bf16_f32 v[pt+4], v[po+8], v[po+9]
  v_cvt_pk_bf16_f32 v[pt+5], v[po+10], v[po+11]
  ds_write_b128 v[239+(\i>>1)], v[pt:pt+3] offset:4608*(\i&1)
  v_cndmask_b32 v[pt+4], v[pt+4], v[po+8], s[52:53]
  ds_write_b64 v[243+(\i>>1)], v[pt+4:pt+5] offset:4608*(\i&1)
.endm
.macro EXPS X, a, n
  .set ei, \a
  .rept \n
  v_exp_f32 v[\X+ei], v[\X+ei]
  .set ei, ei+1
  .endr
.endm
.macro TILE b, qt, pk=0
  .set tX, 128+16*(\qt&1)
  .set tY, 128+16*((\qt+1)&1)
  .set qn, 160+6*((\qt+1)&7)
  .set kb, 208+14*((\b+((\qt+1)>>3))&1)
  .set vc, 208+14*\b
  .if \qt == 0
  .set vp, 208+14*(1-\b)
  .else
  .set vp, vc
  .endif
  .set op, 16*((\qt+7)&7)
  .set oc, 16*\qt
  EXPS tX, 0, 2
  v_smfmac_f32_32x32x32_bf16 v[op:op+15], v[vp+10:vp+13], v[tY+8:tY+15], v236
  EXPS tX, 2, 1
  .if (\qt == 1) && (\pk == 0)
  LOADKV (1-\b)
  .endif
  EXPS tX, 3, 2
  .if (\qt == 7) && (\pk == 0)
  s_waitcnt vmcnt(0)
  .endif
  .if \pk && (\qt == 7)
  .else
  v_mfma_f32_32x32x8_bf16 v[tY:tY+15], v[kb+4:kb+5], v[qn+4:qn+5], 0
  .endif
  EXPS tX, 5, 4
  .if \pk && (\qt == 7)
  .else
  v_mfma_f32_32x32x16_bf16 v[tY:tY+15], v[kb:kb+3], v[qn:qn+3], v[tY:tY+15]
  .endif
  EXPS tX, 9, 4
  v_smfmac_f32_32x32x32_bf16 v[oc:oc+15], v[vc+6:vc+9], v[tX:tX+7], v236
  EXPS tX, 13, 3
  .if \pk && (\qt >= 3)
  PACK (\qt-1)
  .endif
.endm
  v_mbcnt_lo_u32_b32 v236, -1, 0
  v_mbcnt_hi_u32_b32 v236, -1, v236
  v_lshlrev_b32 v237, 4, v236
  v_and_b32 v238, 31, v236
  v_lshrrev_b32 v239, 5, v236
  v_mul_u32_u24 v128, 48, v238
  v_add_u32 v128, %[ql], v128
  v_lshl_add_u32 v129, v239, 3, v128
  v_lshl_add_u32 v128, v239, 4, v128
  s_mov_b32 s52, %[qr]
  .set qi, 0
  .rept 8
  v_add_u32 v[160+6*qi], s52, v128
  v_add_u32 v[164+6*qi], s52, v129
  s_add_u32 s52, s52, 1536
  s_cmp_eq_u32 s52, 12288
  s_cselect_b32 s52, 0, s52
  .set qi, qi+1
  .endr
  v_mad_u32_u24 v239, v239, 20, v238
  v_lshlrev_b32 v239, 4, v239
  v_cmp_eq_u32 vcc, 20, v238
  v_lshlrev_b32 v238, 3, v236
  v_mov_b32 v130, 0x3f803f80
  v_mov_b32 v131, 0x00003f80
  v_cndmask_b32 v136, 0, v130, vcc
  .set qi, 0
  .rept 8
  v_mov_b32 v[214+qi], v136
  v_mov_b32 v[228+qi], v136
  v_mov_b32 v[152+qi], 0
  .set qi, qi+1
  .endr
  v_mov_b32 v212, v130
  v_mov_b32 v226, v130
  v_mov_b32 v213, v131
  v_mov_b32 v227, v131
  .set qi, 0
  .rept 64
  v_mov_b64 v[qi:qi+1], 0
  .set qi, qi+2
  .endr
  v_mov_b32 v236, 0xdddd
  s_mov_b32 s44, -1
  s_mov_b32 s45, 0
  s_mov_b32 s46, 0xfffff
  s_mov_b32 s47, 0xfffff
  s_mov_b32 s40, 0
  .macro WAITCNT n, id, of
  s_mov_b32 s42, 0x100000
Lq\id\()_%=:
  ds_read_b32 v137, v136 offset:\of
  s_waitcnt lgkmcnt(0)
  v_readfirstlane_b32 s43, v137
  s_nop 3
  s_cmp_ge_u32 s43, \n
  s_cbranch_scc1 Lqok\id\()_%=
  s_sleep 1
  s_sub_u32 s42, s42, 1
  s_cmp_lg_u32 s42, 0
  s_cbranch_scc1 Lq\id\()_%=
Lqok\id\()_%=:
  .endm
  v_mov_b32 v136, %[misc]
  .if %c[pa]
  WAITCNT %c[pn], 0, %c[pa]
  .endif
  s_mov_b32 s42, 0x40000
  v_mul_u32_u24 v134, %c[f1s]/16, v237
Lpoll%=:
  global_load_dwordx2 v[132:133], v134, %[fp] sc1
  s_waitcnt vmcnt(0)
  v_cmp_ne_u64 vcc, %[tag], v[132:133]
  s_nop 4
  s_cbranch_vccz Lgo%=
  s_sub_u32 s42, s42, 1
  s_cbranch_scc1 Lgo%=
  s_sleep 8
  s_branch Lpoll%=
Lgo%=:
  LOADKV 0
  WAITCNT 4, 1, 4
  .set qi, 0
  .rept 8
  ds_read_b128 v[160+6*qi:163+6*qi], v[160+6*qi]
  ds_read_b64 v[164+6*qi:165+6*qi], v[164+6*qi] offset:32
  .set qi, qi+1
  .endr
  s_waitcnt vmcnt(0) lgkmcnt(0)
  v_mfma_f32_32x32x8_bf16 v[128:143], v[212:213], v[164:165], 0
  v_mfma_f32_32x32x16_bf16 v[128:143], v[208:211], v[160:163], v[128:143]
  s_mov_b32 s42, 7
  s_nop 15
Lloop%=:
  TILE 0, 0
  TILE 0, 1
  TILE 0, 2
  TILE 0, 3
  TILE 0, 4
  TILE 0, 5
  TILE 0, 6
  TILE 0, 7
  s_sub_u32 s42, s42, 1
  s_cbranch_scc1 Llast%=
  TILE 1, 0
  TILE 1, 1
  TILE 1, 2
  TILE 1, 3
  TILE 1, 4
  TILE 1, 5
  TILE 1, 6
  TILE 1, 7
  s_branch Lloop%=
Llast%=:
  v_mov_b32 v250, %[misc]
  s_mov_b32 s52, 0
  s_mov_b32 s53, -1
  s_mov_b32 s56, 4
  ds_read_b32 v251, v250 offset:40
  TILE 1, 0, 1
  TILE 1, 1, 1
  s_waitcnt lgkmcnt(0)
  v_mov_b32 v160, v206
  v_mov_b32 v206, v251
  SPIN 40, 0, 0
  v_mov_b32 v206, v160
  TILE 1, 2, 1
  TILE 1, 3, 1
  TILE 1, 4, 1
  TILE 1, 5, 1
  TILE 1, 6, 1
  TILE 1, 7, 1
  v_smfmac_f32_32x32x32_bf16 v[112:127], v[232:235], v[152:159], v236
  s_setprio 3
  v_mov_b32 v207, 1
  v_add_u32 v237, %[lr], v237
  v_add_u32 v251, %[lw], v237
  s_mov_b32 s54, 0x3f80
  s_mov_b32 s55, 0x3f800000
  s_nop 7
  PACK 7
  s_mov_b32 s56, %[thr]
  v_mov_b32 v196, v8
  v_mov_b32 v197, v24
  s_waitcnt lgkmcnt(0)
  s_mov_b64 exec, 1
  ds_add_rtn_u32 v206, v250, v207 offset:56
  s_mov_b64 exec, -1
  s_waitcnt lgkmcnt(0)
  v_add_u32 v206, 1, v206
  SPIN 56, 1, 0
  .set k, 0
  .rept 6
  ds_read_b128 v[160+6*k:163+6*k], v243 offset:1536*k
  ds_read_b64 v[164+6*k:165+6*k], v247 offset:1536*k
  .set k, k+1
  .endr
  v_mov_b64 v[200:201], 0
  v_mov_b64 v[202:203], 0
  .set k, 0
  .rept 6
  .set ob, 16*(k/3)
  s_waitcnt lgkmcnt(10-2*k)
  .set e, 0
  .rept 6
  v_dot2c_f32_bf16 v[ob+2*e], s54, v[160+6*k+e]
  v_dot2c_f32_bf16 v[ob+2*e+1], s55, v[160+6*k+e]
  .set e, e+1
  .endr
  v_add_f32 v[196+k/3], v[196+k/3], v[164+6*k]
  .set k, k+1
  .endr
  v_mov_b32 v198, v196
  v_mov_b32 v199, v197
  s_nop 1
  v_permlane32_swap_b32 v196, v198
  v_permlane32_swap_b32 v197, v199
  .set e, 0
  .rept 4
  v_pk_fma_f32 v[200:201], v[2*e:2*e+1], 0, v[200:201] op_sel_hi:[1,0,1]
  v_pk_fma_f32 v[200:201], v[16+2*e:17+2*e], 0, v[200:201] op_sel_hi:[1,0,1]
  .set e, e+1
  .endr
  v_rcp_f32 v204, v198
  v_rcp_f32 v208, v199
  v_pk_fma_f32 v[202:203], v[8:9], 0, v[202:203] op_sel_hi:[1,0,1]
  v_pk_fma_f32 v[202:203], v[10:11], 0, v[202:203] op_sel_hi:[1,0,1]
  v_pk_fma_f32 v[202:203], v[24:25], 0, v[202:203] op_sel_hi:[1,0,1]
  v_pk_fma_f32 v[202:203], v[26:27], 0, v[202:203] op_sel_hi:[1,0,1]
  v_min_f32 v196, v198, v199
  v_max_f32 v197, v198, v199
  v_add_f32 v202, v202, v203
  v_add_f32 v200, v200, v201
  v_cndmask_b32 v202, v202, 0, s[52:53]
  v_fma_f32 v200, v198, 0, v200
  v_fma_f32 v202, v199, 0, v202
  v_cmp_le_f32 vcc, 0x2f800000, v196
  v_add_f32 v200, v200, v202
  s_nop 3
  s_mov_b64 s[48:49], vcc
  v_cmp_ge_f32 vcc, 0x7d800000, v197
  s_nop 3
  s_and_b64 s[48:49], s[48:49], vcc
  v_cmp_eq_f32 vcc, 0, v200
  s_nop 3
  s_and_b64 s[48:49], s[48:49], vcc
  s_cmp_eq_u64 s[48:49], -1
  s_cselect_b32 s43, 1, 0x10001
  s_cmp_eq_u32 s56, 4
  s_cselect_b32 s43, s43, 1
  v_pk_mul_f32 v[8:9], v[8:9], v[204:205] op_sel_hi:[1,0]
  v_pk_mul_f32 v[10:11], v[10:11], v[204:205] op_sel_hi:[1,0]
  v_pk_mul_f32 v[24:25], v[24:25], v[208:209] op_sel_hi:[1,0]
  v_pk_mul_f32 v[26:27], v[26:27], v[208:209] op_sel_hi:[1,0]
  v_mov_b32 v207, s43
  v_cvt_pk_bf16_f32 v164, v8, v9
  v_cvt_pk_bf16_f32 v165, v10, v11
  v_cvt_pk_bf16_f32 v172, v24, v25
  v_cvt_pk_bf16_f32 v173, v26, v27
  s_mov_b64 exec, 0xffffffff
  ds_write_b64 v251, v[164:165]
  ds_write_b64 v251, v[172:173] offset:512
  s_mov_b64 exec, 1
  ds_add_rtn_u32 v206, v250, v207 offset:60
  s_mov_b64 exec, -1
  .set e, 0
  .rept 4
  v_pk_mul_f32 v[2*e:2*e+1], v[2*e:2*e+1], v[204:205] op_sel_hi:[1,0]
  v_pk_mul_f32 v[16+2*e:17+2*e], v[16+2*e:17+2*e], v[208:209] op_sel_hi:[1,0]
  .set e, e+1
  .endr
  .set e, 0
  .rept 4
  v_cvt_pk_bf16_f32 v[160+e], v[2*e], v[2*e+1]
  v_cvt_pk_bf16_f32 v[168+e], v[16+2*e], v[17+2*e]
  .set e, e+1
  .endr
  s_waitcnt lgkmcnt(0)
  v_add_u32 v206, v207, v206
  SPIN 60, 2, 1
  s_lshr_b32 %[redo], s43, 16
  s_cmp_eq_u32 s56, 4
  s_cselect_b32 %[redo], %[redo], 0
  s_cmp_lg_u32 %[redo], 0
  s_cbranch_scc1 Lend%=
  ds_read_b128 v[164:167], v237
  buffer_store_dwordx4 v[160:163], v248, %[ors], 0 offen sc1
  buffer_store_dwordx4 v[168:171], v248, %[ors], 0 offen offset:2048 sc1
  s_waitcnt lgkmcnt(0)
  buffer_store_dwordx4 v[164:167], v249, %[ors], 0 offen sc1
  v_mov_b32 v207, 1
  s_mov_b64 exec, 1
  ds_add_u32 v250, v207 offset:76
  s_mov_b64 exec, -1
Lend%=:
  s_setprio 0
  s_nop 1
.purgem SPIN
.purgem PACK
.purgem LOADKV
.purgem WAITCNT
.purgem EXPS
.purgem TILE
)ASM"
    : [redo] "=&s"(redo)
    : [rs] "s"(rsrc), [fp] "s"(flagp), [tag] "s"(tag), [ql] "s"(qlds), [qr] "s"(qrot), [misc] "s"(misc), [thr] "s"(thr), [ors] "s"(ors),
      "{v[240:243]}"(aw), "{v[244:247]}"(ax), "{v[248:249]}"(ao), [lr] "s"(lr), [lw] "s"(lw), [pa] "i"(POLL_AT), [pn] "i"(POLL_N), [f1s] "i"(F1S)
    : "memory", "vcc", "scc", "s40", "s42", "s43", "s44", "s45", "s46", "s47", "s48", "s49", "s52", "s53", "s54", "s55", "s56",
      "v250", "v251", V0_239);
  return redo;
}

#define V80_139 "v80", "v81", "v82", "v83", "v84", "v85", "v86", "v87", "v88", "v89", "v90", "v91", "v92", "v93", "v94", "v95", "v96", "v97", "v98", "v99", "v100", "v101", "v102", "v103", "v104", "v105", "v106", "v107", "v108", "v109", "v110", "v111", "v112", "v113", "v114", "v115", "v116", "v117", "v118", "v119", "v120", "v121", "v122", "v123", "v124", "v125", "v126", "v127", "v128", "v129", "v130", "v131", "v132", "v133", "v134", "v135", "v136", "v137", "v138", "v139"
#define V0_95 "v0", "v1", "v2", "v3", "v4", "v5", "v6", "v7", "v8", "v9", "v10", "v11", "v12", "v13", "v14", "v15", "v16", "v17", "v18", "v19", "v20", "v21", "v22", "v23", "v24", "v25", "v26", "v27", "v28", "v29", "v30", "v31", "v32", "v33", "v34", "v35", "v36", "v37", "v38", "v39", "v40", "v41", "v42", "v43", "v44", "v45", "v46", "v47", "v48", "v49", "v50", "v51", "v52", "v53", "v54", "v55", "v56", "v57", "v58", "v59", "v60", "v61", "v62", "v63", "v64", "v65", "v66", "v67", "v68", "v69", "v70", "v71", "v72", "v73", "v74", "v75", "v76", "v77", "v78", "v79", "v80", "v81", "v82", "v83", "v84", "v85", "v86", "v87", "v88", "v89", "v90", "v91", "v92", "v93", "v94", "v95"
#define V200_239 "v200", "v201", "v202", "v203", "v204", "v205", "v206", "v207", "v208", "v209", "v210", "v211", "v212", "v213", "v214", "v215", "v216", "v217", "v218", "v219", "v220", "v221", "v222", "v223", "v224", "v225", "v226", "v227", "v228", "v229", "v230", "v231", "v232", "v233", "v234", "v235", "v236", "v237", "v238", "v239"
#define V116_235 "v116", "v117", "v118", "v119", "v120", "v121", "v122", "v123", "v124", "v125", "v126", "v127", "v128", "v129", "v130", "v131", "v132", "v133", "v134", "v135", "v136", "v137", "v138", "v139", "v140", "v141", "v142", "v143", "v144", "v145", "v146", "v147", "v148", "v149", "v150", "v151", "v152", "v153", "v154", "v155", "v156", "v157", "v158", "v159", "v160", "v161", "v162", "v163", "v164", "v165", "v166", "v167", "v168", "v169", "v170", "v171", "v172", "v173", "v174", "v175", "v176", "v177", "v178", "v179", "v180", "v181", "v182", "v183", "v184", "v185", "v186", "v187", "v188", "v189", "v190", "v191", "v192", "v193", "v194", "v195", "v196", "v197", "v198", "v199", "v200", "v201", "v202", "v203", "v204", "v205", "v206", "v207", "v208", "v209", "v210", "v211", "v212", "v213", "v214", "v215", "v216", "v217", "v218", "v219", "v220", "v221", "v222", "v223", "v224", "v225", "v226", "v227", "v228", "v229", "v230", "v231", "v232", "v233", "v234", "v235"
typedef short s16x4v __attribute__((ext_vector_type(4)));

__device__ __forceinline__ void ffn1_core(f32x16 (&acc)[4], u32x4 rsrc, unsigned ring, unsigned vt0, unsigned vt1, unsigned vx, unsigned voff,
                                          u32x4 rwo, u32x16& toa, u32x4& tob, u32x4& toc, u32x4& tod, const void* tab, unsigned vtab,
                                          u32x4 ors, unsigned gc0, unsigned vfill) {
  asm volatile(R"ASM(
.macro GAP nf
  .if (%c[pad] > \nf) && (si < %c[lo])
  s_nop (%c[pad]-\nf-1)
  .endif
.endm
.macro DMA1 st
  s_add_u32 m0, %[ring], (\st&7)*2048
  s_add_u32 s41, s40, 26624
  buffer_load_dwordx4 %[voff], %[rs], s40 offen lds
  buffer_load_dwordx4 %[voff], %[rs], s41 offen offset:1024 lds
  s_add_u32 s40, s40, 55296
.endm
.macro RDA0 st
  ds_read_b64_tr_b16 v[64+16*(\st&1):65+16*(\st&1)], %[vt0] offset:(\st&7)*2048
  ds_read_b64_tr_b16 v[66+16*(\st&1):67+16*(\st&1)], %[vt0] offset:(\st&7)*2048+1024
.endm
.macro RDA1 st
  ds_read_b64_tr_b16 v[68+16*(\st&1):69+16*(\st&1)], %[vt1] offset:(\st&7)*2048
  ds_read_b64_tr_b16 v[70+16*(\st&1):71+16*(\st&1)], %[vt1] offset:(\st&7)*2048+1024
.endm
.macro RDB st
  ds_read_b128 v[72+16*(\st&1):75+16*(\st&1)], %[vx] offset:\st*32
  ds_read_b128 v[76+16*(\st&1):79+16*(\st&1)], %[vx] offset:\st*32+41472
.endm
.macro MM d, a, b, st
  .if \st == 0
  v_mfma_f32_32x32x16_bf16 v[\d:\d+15], v[\a:\a+3], v[\b:\b+3], 0
  .else
  v_mfma_f32_32x32x16_bf16 v[\d:\d+15], v[\a:\a+3], v[\b:\b+3], v[\d:\d+15]
  .endif
.endm
.macro STEP st
  .set pb, 64+16*(\st&1)
  .if \st == %c[lo]
  s_setprio 0
  .endif
  .if \st < 7
  s_waitcnt vmcnt(19) lgkmcnt(2)
  .elseif (\st > 8) && (\st <= 15)
  s_waitcnt vmcnt(17) lgkmcnt(2)
  .elseif \st < 39
  s_waitcnt vmcnt(12) lgkmcnt(2)
  .else
  s_waitcnt lgkmcnt(2)
  .endif
  MM 0, pb, (pb+8), \st
  .if \st < 39
  RDA0 (\st+1)
  .endif
  GAP 2
  MM 16, pb, (pb+12), \st
  .if \st < 39
  RDB (\st+1)
  s_waitcnt lgkmcnt(4)
  .else
  s_waitcnt lgkmcnt(0)
  .endif
  GAP 3
  MM 32, (pb+4), (pb+8), \st
  .if \st < 39
  RDA1 (\st+1)
  .endif
  GAP 2
  MM 48, (pb+4), (pb+12), \st
  .if \st < 32
  DMA1 \st
  .if \st == 8
  .rept 5
  s_lshr_b32 s43, s44, 2
  s_and_b32 s45, s44, 3
  s_lshl_b32 s43, s43, 12
  s_lshl_b32 s45, s45, 8
  s_or_b32 s43, s43, s45
  buffer_store_dwordx4 v[126:129], %[vfill], %[ors], s43 offen sc1
  s_add_u32 s44, s44, 1
  .endr
  .endif
  GAP 6
  .else
  s_add_u32 m0, %[ring], (\st&7)*2048
  s_nop 0
  buffer_load_dwordx4 v[96+2*(\st&7)], %[rwo], 0 offen lds
  .if \st < 39
  s_add_u32 m0, %[ring], (\st&7)*2048+1024
  s_nop 0
  buffer_load_dwordx4 v[97+2*(\st&7)], %[rwo], 0 offen lds
  .endif
  GAP 7
  .endif
.endm
  s_mov_b32 s42, m0
  s_mov_b32 s40, 0
  .set si, 0
  .rept 8
  DMA1 si
  .set si, si+1
  .endr
  v_add_u32 v124, 4096, %[vtab]
  global_load_dwordx4 v[96:99], %[vtab], %[tab]
  global_load_dwordx4 v[100:103], %[vtab], %[tab] offset:1024
  global_load_dwordx4 v[104:107], %[vtab], %[tab] offset:2048
  global_load_dwordx4 v[108:111], %[vtab], %[tab] offset:3072
  global_load_dwordx4 v[112:115], v124, %[tab]
  global_load_dwordx4 v[116:119], v124, %[tab] offset:1024
  global_load_dwordx4 v[120:123], v124, %[tab] offset:2048
  v_mov_b64 v[126:127], -1
  v_mov_b64 v[128:129], -1
  s_mov_b32 s44, %[gc0]
  s_waitcnt vmcnt(21)
  RDA0 0
  RDB 0
  RDA1 0
  .set si, 0
  .rept 40
  STEP si
  .set si, si+1
  .endr
  s_setprio 1
  s_mov_b32 m0, s42
  s_nop 15
.purgem GAP
.purgem DMA1
.purgem RDA0
.purgem RDA1
.purgem RDB
.purgem MM
.purgem STEP
)ASM"
    : "=&{v[0:15]}"(acc[0]), "=&{v[16:31]}"(acc[1]), "=&{v[32:47]}"(acc[2]), "=&{v[48:63]}"(acc[3]),
      "=&{v[96:111]}"(toa), "=&{v[112:115]}"(tob), "=&{v[116:119]}"(toc), "=&{v[120:123]}"(tod)
    : [rs] "s"(rsrc), [ring] "s"(ring), [vt0] "v"(vt0), [vt1] "v"(vt1), [vx] "v"(vx), [voff] "v"(voff), [rwo] "s"(rwo), [tab] "s"(tab), [vtab] "v"(vtab),
      [pad] "i"(PAD1), [lo] "i"(LOW1), [ors] "s"(ors), [gc0] "s"(gc0), [vfill] "v"(vfill)
    : "memory", "scc", "s40", "s41", "s42", "s43", "s44", "s45", "v124", "v126", "v127", "v128", "v129",
      "v64", "v65", "v66", "v67", "v68", "v69", "v70", "v71", "v72", "v73", "v74", "v75", "v76", "v77", "v78", "v79",
      "v80", "v81", "v82", "v83", "v84", "v85", "v86", "v87", "v88", "v89", "v90", "v91", "v92", "v93", "v94", "v95");
}

__device__ __forceinline__ void ffn2_core(f32x16 (&acc)[5], u32x4 rw, u32x4 rh, unsigned ring, unsigned ns, unsigned vwr,
                                          unsigned w0, unsigned w1, unsigned w2, unsigned w3, unsigned w4, unsigned vh) {
  asm volatile(R"ASM(
.macro RDW n, hi
  .if (\n) < 70
  ds_read_b64_tr_b16 v[128+4*((\n)%%3)+2*\hi:129+4*((\n)%%3)+2*\hi], %[vwr] offset:(((\n)/5)%%3)*5120+((\n)%%5)*32+1280*\hi
  .endif
.endm
.macro LDH i, t
  buffer_load_dwordx4 v[80+16*((\i)%%3)+4*\t:83+16*((\i)%%3)+4*\t], %[vh], %[rh], s41 offen offset:1024*\t sc1
.endm
.macro MM i, c, t
  .set md, 16*\c+4*\t
  .set ma, 128+4*((5*\i+\c)%%3)
  .set mb, 80+16*(\i%%3)+4*\t
  .if \i == 0
  v_mfma_f32_16x16x32_bf16 v[md:md+3], v[ma:ma+3], v[mb:mb+3], 0
  .else
  v_mfma_f32_16x16x32_bf16 v[md:md+3], v[ma:ma+3], v[mb:mb+3], v[md:md+3]
  .endif
.endm
.macro HEAD i, c, vm
  .if (5*\i+\c) == 69
  s_waitcnt lgkmcnt(0)
  .elseif \vm
  s_waitcnt vmcnt(9) lgkmcnt(2)
  .else
  s_waitcnt lgkmcnt(2)
  .endif
  MM \i, \c, 0
  RDW (5*\i+\c+2), 0
  MM \i, \c, 1
  RDW (5*\i+\c+2), 1
  MM \i, \c, 2
.endm
.macro STEP i
  .set sl, (\i+2)%%3
  HEAD \i, 0, 0
  s_add_u32 m0, %[ring], sl*5120
  s_nop 0
  buffer_load_dwordx4 %[w0], %[rw], s40 offen lds
  MM \i, 0, 3
  buffer_load_dwordx4 %[w1], %[rw], s40 offen offset:1024 lds
  buffer_load_dwordx4 %[w2], %[rw], s40 offen offset:2048 lds
  HEAD \i, 1, 0
  buffer_load_dwordx4 %[w3], %[rw], s40 offen offset:3072 lds
  s_add_u32 m0, %[ring], sl*5120+4096
  MM \i, 1, 3
  buffer_load_dwordx4 %[w4], %[rw], s40 offen lds
  s_add_u32 s40, s40, 40960
  HEAD \i, 2, 0
  LDH (\i+2), 0
  LDH (\i+2), 1
  MM \i, 2, 3
  LDH (\i+2), 2
  LDH (\i+2), 3
  HEAD \i, 3, 1
  s_add_u32 s41, s41, 4096
  MM \i, 3, 3
  HEAD \i, 4, 0
  MM \i, 4, 3
.endm
  s_mov_b32 s42, m0
  s_mov_b32 s41, 0
  LDH 0, 0
  LDH 0, 1
  LDH 0, 2
  LDH 0, 3
  s_mov_b32 s41, 4096
  s_mov_b32 s40, 81920
  LDH 1, 0
  LDH 1, 1
  LDH 1, 2
  LDH 1, 3
  s_mov_b32 s41, 8192
  RDW 0, 0
  RDW 0, 1
  RDW 1, 0
  RDW 1, 1
  s_waitcnt vmcnt(4)
  .set si, 0
  .rept 13
  STEP si
  .set si, si+1
  .endr
  s_cmp_eq_u32 %[ns], 13
  s_cbranch_scc1 Lend%=
  STEP 13
Lend%=:
  s_waitcnt vmcnt(0) lgkmcnt(0)
  s_mov_b32 m0, s42
  s_nop 7
.purgem RDW
.purgem LDH
.purgem MM
.purgem HEAD
.purgem STEP
)ASM"
    : "=&{v[0:15]}"(acc[0]), "=&{v[16:31]}"(acc[1]), "=&{v[32:47]}"(acc[2]), "=&{v[48:63]}"(acc[3]), "=&{v[64:79]}"(acc[4])
    : [rw] "s"(rw), [rh] "s"(rh), [ring] "s"(ring), [ns] "s"(ns), [vwr] "v"(vwr), [w0] "v"(w0), [w1] "v"(w1), [w2] "v"(w2), [w3] "v"(w3), [w4] "v"(w4),
      [vh] "v"(vh)
    : "memory", "scc", "s40", "s41", "s42", V80_139);
}

struct WoF { u32x16 a, b, c, d, e, f; u32x4 g; };
__device__ __forceinline__ void wo_frags_a(WoF& w, unsigned rd) {
  asm volatile(R"ASM(
  .set fi, 0
  .rept 15
  ds_read_b64_tr_b16 v[140+4*fi:141+4*fi], %4 offset:5120*(fi/5)+32*(fi%%5)
  ds_read_b64_tr_b16 v[142+4*fi:143+4*fi], %4 offset:5120*(fi/5)+32*(fi%%5)+1280
  .if (fi%%5) == 4
  s_waitcnt lgkmcnt(4)
  .endif
  .set fi, fi+1
  .endr
  s_waitcnt lgkmcnt(0)
)ASM" : "=&{v[140:155]}"(w.a), "=&{v[156:171]}"(w.b), "=&{v[172:187]}"(w.c), "=&{v[188:203]}"(w.d) : "v"(rd) : "memory");
}
__device__ __forceinline__ void wo_frags_b(WoF& w, unsigned rd) {
  asm volatile(R"ASM(
  .set fi, 0
  .rept 10
  ds_read_b64_tr_b16 v[200+4*fi:201+4*fi], %4 offset:5120*(1+fi/5)+32*(fi%%5)
  ds_read_b64_tr_b16 v[202+4*fi:203+4*fi], %4 offset:5120*(1+fi/5)+32*(fi%%5)+1280
  .if fi == 4
  s_waitcnt lgkmcnt(4)
  .endif
  .set fi, fi+1
  .endr
  s_waitcnt lgkmcnt(0)
)ASM" : "+{v[188:203]}"(w.d), "=&{v[204:219]}"(w.e), "=&{v[220:235]}"(w.f), "=&{v[236:239]}"(w.g) : "v"(rd) : "memory");
}
__device__ __forceinline__ void wo_core(f32x16 (&acc)[5], const WoF& w, u32x4 ro, u32x4 rx, u32x4 rout, unsigned gw, unsigned cnt,
                                        unsigned vo, unsigned vl, unsigned vx) {
  asm volatile(R"ASM(
.macro LDO s
  s_mov_b32 s40, 4096*\s
  buffer_load_dwordx4 v[80+16*(\s%%3):83+16*(\s%%3)], %[vo], %[ro], s40 offen sc1
  buffer_load_dwordx4 v[84+16*(\s%%3):87+16*(\s%%3)], %[vo], %[ro], s40 offen offset:1024 sc1
  buffer_load_dwordx4 v[88+16*(\s%%3):91+16*(\s%%3)], %[vo], %[ro], s40 offen offset:2048 sc1
  buffer_load_dwordx4 v[92+16*(\s%%3):95+16*(\s%%3)], %[vo], %[ro], s40 offen offset:3072 sc1
.endm
.macro MX s, k
  .set ob_, 80+16*(\s%%3)
  .if \k == 0
  v_max_u32 v138, v[ob_], v[ob_+1]
  .elseif \k < 8
  v_max3_u32 v138, v138, v[ob_+2*\k], v[ob_+2*\k+1]
  .else
  v_cmp_eq_u32 vcc, -1, v138
  .endif
.endm
.macro VER s
  s_cbranch_vccz 4f
  s_mov_b32 s41, 0x20000
3:
  s_sleep 2
  LDO \s
  .if \s < 4
  LDO (\s+1)
  .endif
  .if \s < 3
  LDO (\s+2)
  .endif
  .if \s == 4
  s_waitcnt vmcnt(0)
  .elseif \s == 3
  s_waitcnt vmcnt(4)
  .else
  s_waitcnt vmcnt(8)
  .endif
  .set k, 0
  .rept 9
  MX \s, k
  .set k, k+1
  .endr
  s_sub_u32 s41, s41, 1
  s_cbranch_scc1 4f
  s_nop 3
  s_cbranch_vccnz 3b
4:
.endm
.macro STEP s, n
  VER \s
  .if \s == 0
  s_setprio 2
  .endif
  .set mi, 0
  .rept 20
  v_mfma_f32_16x16x32_bf16 v[4*mi:4*mi+3], v[140+20*\s+4*(mi/4):143+20*\s+4*(mi/4)], v[80+16*(\s%%3)+4*(mi%%4):83+16*(\s%%3)+4*(mi%%4)], v[4*mi:4*mi+3]
  .if \s < 4
  .if mi == 8
  s_waitcnt vmcnt(\n)
  .endif
  .if (mi >= 9) && (mi < 18)
  MX (\s+1), (mi-9)
  .endif
  .endif
  .set mi, mi+1
  .endr
.endm
.macro FIN g
  .set t, 0
  .rept 4
  .if t != \g
  .set c, 0
  .rept 5
  ds_write_b128 %[vl], v[16*c+4*t:16*c+4*t+3] offset:16384*\g+5120*((t-\g-1)&3)+1024*c
  .set c, c+1
  .endr
  .endif
  .set t, t+1
  .endr
  .set c, 0
  .rept 5
  v_lshlrev_b32 v140, 16, v[128+2*c]
  v_and_b32 v141, 0xffff0000, v[128+2*c]
  v_lshlrev_b32 v142, 16, v[129+2*c]
  v_and_b32 v143, 0xffff0000, v[129+2*c]
  v_pk_add_f32 v[16*c+4*\g:16*c+4*\g+1], v[16*c+4*\g:16*c+4*\g+1], v[140:141]
  v_pk_add_f32 v[16*c+4*\g+2:16*c+4*\g+3], v[16*c+4*\g+2:16*c+4*\g+3], v[142:143]
  .set c, c+1
  .endr
  s_waitcnt lgkmcnt(0)
  s_mov_b64 exec, 1
  ds_add_rtn_u32 v138, v139, v144
  s_mov_b64 exec, -1
  s_waitcnt lgkmcnt(0)
  v_add_u32 v138, 1, v138
  s_mov_b32 s41, 0x100000
  s_branch 2f
1:
  ds_read_b32 v138, v139
  s_waitcnt lgkmcnt(0)
  s_sub_u32 s41, s41, 1
  s_cbranch_scc1 3f
2:
  v_readfirstlane_b32 s43, v138
  s_nop 3
  s_cmp_ge_u32 s43, 4
  s_cbranch_scc0 1b
3:
  .set c, 0
  .rept 5
  .set r, 0
  .rept 3
  ds_read_b128 v[148+12*c+4*r:151+12*c+4*r], %[vl] offset:16384*((\g-1-r)&3)+5120*r+1024*c
  .set r, r+1
  .endr
  .set c, c+1
  .endr
  .set c, 0
  .rept 5
  s_waitcnt lgkmcnt(12-3*c)
  v_pk_add_f32 v[148+12*c:149+12*c], v[148+12*c:149+12*c], v[16*c+4*\g:16*c+4*\g+1]
  v_pk_add_f32 v[150+12*c:151+12*c], v[150+12*c:151+12*c], v[16*c+4*\g+2:16*c+4*\g+3]
  v_pk_add_f32 v[152+12*c:153+12*c], v[152+12*c:153+12*c], v[156+12*c:157+12*c]
  v_pk_add_f32 v[154+12*c:155+12*c], v[154+12*c:155+12*c], v[158+12*c:159+12*c]
  v_pk_add_f32 v[148+12*c:149+12*c], v[148+12*c:149+12*c], v[152+12*c:153+12*c]
  v_pk_add_f32 v[150+12*c:151+12*c], v[150+12*c:151+12*c], v[154+12*c:155+12*c]
  s_nop 0
  v_cvt_pk_bf16_f32 v[148+12*c], v[148+12*c], v[149+12*c]
  v_cvt_pk_bf16_f32 v[149+12*c], v[150+12*c], v[151+12*c]
  buffer_store_dwordx2 v[148+12*c:149+12*c], %[vx], %[rout], 0 offen offset:32*c
  .set c, c+1
  .endr
.endm
  buffer_load_dwordx2 v[128:129], %[vx], %[rx], 0 offen
  buffer_load_dwordx2 v[130:131], %[vx], %[rx], 0 offen offset:32
  buffer_load_dwordx2 v[132:133], %[vx], %[rx], 0 offen offset:64
  buffer_load_dwordx2 v[134:135], %[vx], %[rx], 0 offen offset:96
  buffer_load_dwordx2 v[136:137], %[vx], %[rx], 0 offen offset:128
  v_mov_b32 v139, %[cnt]
  s_mov_b32 s41, 0x400000
Lpoll%=:
  ds_read_b32 v138, v139 offset:32
  s_waitcnt lgkmcnt(0)
  v_readfirstlane_b32 s43, v138
  s_sub_u32 s41, s41, 1
  s_cbranch_scc1 Lgo%=
  s_cmp_ge_u32 s43, 4
  s_cbranch_scc1 Lgo%=
  s_sleep 1
  s_branch Lpoll%=
Lgo%=:
  s_waitcnt vmcnt(0)
  s_sleep 10
  LDO 0
  LDO 1
  LDO 2
  s_waitcnt vmcnt(8)
  .set k, 0
  .rept 9
  MX 0, k
  .set k, k+1
  .endr
  s_nop 4
  STEP 0, 4
  LDO 3
  STEP 1, 4
  LDO 4
  STEP 2, 4
  STEP 3, 0
  STEP 4, 0
  v_mov_b32 v144, 1
  s_cmp_eq_u32 %[gw], 1
  s_cbranch_scc1 Lf1_%=
  s_cmp_eq_u32 %[gw], 2
  s_cbranch_scc1 Lf2_%=
  s_cmp_eq_u32 %[gw], 3
  s_cbranch_scc1 Lf3_%=
  s_nop 1
  FIN 0
  s_branch Lend%=
Lf1_%=:
  s_nop 5
  FIN 1
  s_branch Lend%=
Lf2_%=:
  s_nop 3
  FIN 2
  s_branch Lend%=
Lf3_%=:
  s_nop 1
  FIN 3
Lend%=:
.purgem LDO
.purgem MX
.purgem VER
.purgem STEP
.purgem FIN
)ASM"
    : "+{v[0:15]}"(acc[0]), "+{v[16:31]}"(acc[1]), "+{v[32:47]}"(acc[2]), "+{v[48:63]}"(acc[3]), "+{v[64:79]}"(acc[4])
    : "{v[140:155]}"(w.a), "{v[156:171]}"(w.b), "{v[172:187]}"(w.c), "{v[188:203]}"(w.d), "{v[204:219]}"(w.e), "{v[220:235]}"(w.f), "{v[236:239]}"(w.g),
      [ro] "s"(ro), [rx] "s"(rx), [rout] "s"(rout), [gw] "s"(gw), [cnt] "s"(cnt),
      [vo] "v"(vo), [vl] "v"(vl), [vx] "v"(vx)
    : "memory", "vcc", "scc", "s40", "s41", "s43", V80_139);
}

__device__ __forceinline__ void q_core(unsigned vw, unsigned vx, unsigned vex, unsigned vexr,
                                       unsigned q0, unsigned q1, unsigned q2, unsigned q3, unsigned q4,
                                       unsigned misc, unsigned pairc, unsigned kh, float scale) {
  asm volatile(R"ASM(
.macro OFFS u
  .set ch_, (\u)>>1
  .if ch_ == 0
  .set wo_, 25600
  .elseif ch_ == 1
  .set wo_, 46080
  .elseif ch_ == 2
  .set wo_, 0
  .elseif ch_ == 3
  .set wo_, 25600
  .else
  .set wo_, 46080
  .endif
  .set wo_, wo_+5120*((\u)&1)
  .set xo_, 256*ch_+64*((\u)&1)
.endm
.macro RDW u, t
  OFFS \u
  ds_read_b64_tr_b16 v[40+28*((\u)&1)+4*\t:41+28*((\u)&1)+4*\t], %0 offset:wo_+32*\t
  ds_read_b64_tr_b16 v[42+28*((\u)&1)+4*\t:43+28*((\u)&1)+4*\t], %0 offset:wo_+32*\t+1280
.endm
.macro RDX u
  OFFS \u
  ds_read_b128 v[60+28*((\u)&1):63+28*((\u)&1)], %1 offset:xo_
  ds_read_b128 v[64+28*((\u)&1):67+28*((\u)&1)], %1 offset:xo_+20736
.endm
.macro PRE u
  RDX \u
  RDW \u, 0
  RDW \u, 1
  RDW \u, 2
  RDW \u, 3
  RDW \u, 4
.endm
.macro M1 u, d, a, b
  .if \u == 0
  v_mfma_f32_16x16x32_bf16 v[\d:\d+3], v[\a:\a+3], v[\b:\b+3], 0
  .else
  v_mfma_f32_16x16x32_bf16 v[\d:\d+3], v[\a:\a+3], v[\b:\b+3], v[\d:\d+3]
  .endif
.endm
.macro RDC of, both
  .if \both
  ds_read_b64 v[126:127], v124 offset:\of
  .else
  ds_read_b32 v126, v124 offset:\of
  .endif
.endm
.macro CHK of, both, n, id
  s_mov_b32 s41, 0x100000
  s_branch Lc\id\()_%=
Lw\id\()_%=:
  RDC \of, \both
  s_waitcnt lgkmcnt(0)
  s_sub_u32 s41, s41, 1
  s_cbranch_scc1 Lx\id\()_%=
Lc\id\()_%=:
  .if \both
  v_lshrrev_b32 v127, 1, v127
  v_min_u32 v126, v126, v127
  s_nop 0
  .endif
  v_readfirstlane_b32 s43, v126
  s_nop 3
  s_cmp_ge_u32 s43, \n
  s_cbranch_scc0 Lw\id\()_%=
Lx\id\()_%=:
.endm
.macro ARR reg, of
  s_mov_b64 exec, 1
  ds_add_u32 \reg, v125 offset:\of
  s_mov_b64 exec, -1
.endm
.macro MM u, t, last, ex
  .if \last
  s_waitcnt lgkmcnt(2*(4-\t))
  .elseif \t == 0
  s_waitcnt lgkmcnt(8+\ex)
  .else
  s_waitcnt lgkmcnt(10+\ex)
  .endif
  M1 \u, (4*\t), (40+28*((\u)&1)+4*\t), (60+28*((\u)&1))
  .if (\t == 0) && (\u == 5)
  CHK 12, 0, 2, 5
  .endif
  .if (\t == 0) && (\u == 7)
  CHK 16, 1, 2, 7
  .endif
  .if (\last == 0) && (\t == 0)
  RDX (\u+1)
  .endif
  M1 \u, (20+4*\t), (40+28*((\u)&1)+4*\t), (64+28*((\u)&1))
  .if \last == 0
  RDW (\u+1), \t
  .endif
.endm
.macro STEP u, last, ex
  MM \u, 0, \last, \ex
  MM \u, 1, \last, \ex
  MM \u, 2, \last, \ex
  MM \u, 3, \last, \ex
  MM \u, 4, \last, \ex
.endm
.macro FIN sb, kb
  ds_write_b128 %2, v[\sb:\sb+3]
  ds_write_b128 %2, v[\sb+4:\sb+7] offset:1024
  ds_write_b128 %2, v[\sb+8:\sb+11] offset:2048
  ds_write_b128 %2, v[\sb+12:\sb+15] offset:3072
  ds_write_b128 %2, v[\sb+16:\sb+19] offset:4096
  s_waitcnt lgkmcnt(0)
  s_mov_b64 exec, 1
  ds_add_rtn_u32 v126, v124, v125
  s_mov_b64 exec, -1
  s_waitcnt lgkmcnt(0)
  v_add_u32 v126, 1, v126
  CHK 0, 0, 2, 2\sb
  ds_read_b128 v[40:43], %3
  ds_read_b128 v[44:47], %3 offset:1024
  ds_read_b128 v[48:51], %3 offset:2048
  ds_read_b128 v[52:55], %3 offset:3072
  ds_read_b128 v[56:59], %3 offset:4096
  .set ft, 0
  .rept 5
  s_waitcnt lgkmcnt(4)
  v_pk_add_f32 v[40+4*ft:41+4*ft], v[40+4*ft:41+4*ft], v[\kb+4*ft:\kb+1+4*ft]
  v_pk_add_f32 v[42+4*ft:43+4*ft], v[42+4*ft:43+4*ft], v[\kb+2+4*ft:\kb+3+4*ft]
  v_pk_mul_f32 v[40+4*ft:41+4*ft], v[40+4*ft:41+4*ft], v[60:61]
  v_pk_mul_f32 v[42+4*ft:43+4*ft], v[42+4*ft:43+4*ft], v[60:61]
  s_nop 0
  v_cvt_pk_bf16_f32 v[40+4*ft], v[40+4*ft], v[41+4*ft]
  v_cvt_pk_bf16_f32 v[41+4*ft], v[42+4*ft], v[43+4*ft]
  .if ft == 0
  ds_write_b64 %4, v[40:41]
  .elseif ft == 1
  ds_write_b64 %5, v[44:45]
  .elseif ft == 2
  ds_write_b64 %6, v[48:49]
  .elseif ft == 3
  ds_write_b64 %7, v[52:53]
  .else
  ds_write_b64 %8, v[56:57]
  .endif
  .set ft, ft+1
  .endr
.endm
  v_mov_b32 v124, %9
  v_mov_b32 v125, 1
  RDC 28, 0
  s_waitcnt lgkmcnt(0)
  CHK 28, 0, 8, 0
  PRE 0
  STEP 0, 0, 0
  STEP 1, 0, 0
  STEP 2, 0, 0
  s_waitcnt lgkmcnt(0)
  ARR v124, 24
  STEP 3, 1, 0
  RDC 8, 0
  s_waitcnt lgkmcnt(0)
  CHK 8, 0, 2, 3
  PRE 4
  RDC 12, 0
  STEP 4, 0, 1
  STEP 5, 0, 0
  ARR v124, 20
  RDC 16, 1
  STEP 6, 0, 2
  STEP 7, 0, 0
  STEP 8, 0, 0
  STEP 9, 1, 0
  v_mov_b32 v124, %10
  v_mov_b32 v60, %12
  v_mov_b32 v61, %12
  s_cmp_eq_u32 %11, 0
  s_nop 4
  s_cbranch_scc0 Lk1_%=
  FIN 20, 0
  s_branch Lfe_%=
Lk1_%=:
  FIN 0, 20
Lfe_%=:
  v_mov_b32 v124, %9
  s_waitcnt lgkmcnt(0)
  s_mov_b64 exec, 1
  ds_add_rtn_u32 v126, v124, v125 offset:4
  s_mov_b64 exec, -1
  s_waitcnt lgkmcnt(0)
  v_add_u32 v126, 1, v126
  CHK 4, 0, 4, 9
.purgem OFFS
.purgem RDW
.purgem RDX
.purgem PRE
.purgem M1
.purgem RDC
.purgem CHK
.purgem ARR
.purgem MM
.purgem STEP
.purgem FIN
)ASM"
    :
    : "v"(vw), "v"(vx), "v"(vex), "v"(vexr), "v"(q0), "v"(q1), "v"(q2), "v"(q3), "v"(q4), "s"(misc), "s"(pairc), "s"(kh), "s"(scale)
    : "memory", "scc", "s41", "s43", "v124", "v125", "v126", "v127", V0_95);
}

__device__ __forceinline__ f32x4 kv_core(unsigned vw, unsigned vx, unsigned isv) {
  f32x4 acc;
  asm volatile(R"ASM(
.macro RD s
  ds_read_b64_tr_b16 v[8+8*(\s%%6):9+8*(\s%%6)], %1 offset:1280*\s
  ds_read_b64_tr_b16 v[10+8*(\s%%6):11+8*(\s%%6)], %1 offset:1280*\s+320
  ds_read_b128 v[12+8*(\s%%6):15+8*(\s%%6)], %2 offset:64*\s
.endm
.macro ST s, oa, ob
  .if \s < 16
  s_waitcnt lgkmcnt(12)
  .else
  s_waitcnt lgkmcnt(3*(19-\s))
  .endif
  .if \s == 0
  v_mfma_f32_16x16x32_bf16 v[0:3], v[\oa+8*(\s%%6):\oa+3+8*(\s%%6)], v[\ob+8*(\s%%6):\ob+3+8*(\s%%6)], 0
  .else
  v_mfma_f32_16x16x32_bf16 v[0:3], v[\oa+8*(\s%%6):\oa+3+8*(\s%%6)], v[\ob+8*(\s%%6):\ob+3+8*(\s%%6)], v[0:3]
  .endif
  .if \s < 15
  RD (\s+5)
  .endif
.endm
.macro ALL oa, ob
  .set ks, 0
  .rept 20
  ST ks, \oa, \ob
  .set ks, ks+1
  .endr
.endm
  RD 0
  RD 1
  RD 2
  RD 3
  RD 4
  s_cmp_eq_u32 %3, 0
  s_cbranch_scc1 Lk%=
  ALL 12, 8
  s_branch Le%=
Lk%=:
  ALL 8, 12
Le%=:
  s_nop 7
.purgem RD
.purgem ST
.purgem ALL
)ASM"
    : "=&{v[0:3]}"(acc) : "v"(vw), "v"(vx), "s"(isv)
    : "memory", "scc", "v8", "v9", "v10", "v11", "v12", "v13", "v14", "v15", "v16", "v17", "v18", "v19", "v20", "v21", "v22", "v23",
      "v24", "v25", "v26", "v27", "v28", "v29", "v30", "v31", "v32", "v33", "v34", "v35", "v36", "v37", "v38", "v39",
      "v40", "v41", "v42", "v43", "v44", "v45", "v46", "v47", "v48", "v49", "v50", "v51", "v52", "v53", "v54", "v55");
  return acc;
}
__device__ __forceinline__ void arrive(unsigned addr) {
  asm volatile("s_waitcnt lgkmcnt(0)\n\ts_mov_b64 exec, 1\n\tds_add_u32 %0, %1\n\ts_mov_b64 exec, -1" :: "v"(addr), "v"(1u) : "memory");
}
template <int N> __device__ __forceinline__ void await(unsigned addr) {
  unsigned t, it, v;
  asm volatile("s_mov_b32 %1, 0x100000\n1:\n\tds_read_b32 %2, %3\n\ts_waitcnt lgkmcnt(0)\n\tv_readfirstlane_b32 %0, %2\n\ts_nop 3\n\ts_cmp_ge_u32 %0, %c4\n\t"
               "s_cbranch_scc1 2f\n\ts_sub_u32 %1, %1, 1\n\ts_cmp_lg_u32 %1, 0\n\ts_cbranch_scc1 1b\n2:"
               : "=&s"(t), "=&s"(it), "=&v"(v) : "v"(addr), "i"(N) : "memory", "scc");
}
__device__ __forceinline__ unsigned pin(unsigned a) { asm volatile("" : "+v"(a)); return a; }
__device__ __forceinline__ void touch(const void* p, unsigned lds) {
  unsigned sv;
  asm volatile("s_mov_b32 %0, m0\n\ts_mov_b32 m0, %2\n\ts_nop 0\n\tglobal_load_lds_dword %1, off\n\ts_mov_b32 m0, %0" : "=&s"(sv) : "v"(p), "s"(lds) : "memory");
}
__device__ __forceinline__ u32x4 mk_rsrc(const void* p, unsigned bytes) {
  return (u32x4){(unsigned)(u64)p, (unsigned)((u64)p >> 32), bytes, 0x00020000u};
}

__device__ __forceinline__ int row_group(int b) { return ((b & 7) >> 1) * 8 + (b >> 5); }
__device__ __forceinline__ int col_slice(int b) { return (b & 1) * 4 + ((b >> 3) & 3); }
template <bool ISA>
__device__ __forceinline__ void role(const u16* __restrict__ x, const u16* __restrict__ wq, const u16* __restrict__ wk,
                                     const u16* __restrict__ wv, const u16* __restrict__ wo, const u16* __restrict__ w1,
                                     const u16* __restrict__ w2, u16* __restrict__ out, char* __restrict__ ws, const int w) {
  extern __shared__ __attribute__((aligned(16))) char smem[];
  const unsigned lb = (unsigned)(uintptr_t)smem;
  const int b = blockIdx.x, rg = row_group(b), j = col_slice(b);
  const int lane = fresh_lane();
  const int i16 = lane & 15, g4 = lane >> 4, tq = i16 >> 2, tp = lane & 3;

  u32x4 xv[10];
  asm volatile("" : "={v[200:203]}"(xv[0]), "={v[204:207]}"(xv[1]), "={v[208:211]}"(xv[2]), "={v[212:215]}"(xv[3]), "={v[216:219]}"(xv[4]),
               "={v[220:223]}"(xv[5]), "={v[224:227]}"(xv[6]), "={v[228:231]}"(xv[7]), "={v[232:235]}"(xv[8]), "={v[236:239]}"(xv[9]));
  const char* wqb = (const char*)wq + j * 160;
  unsigned woff[5];
#pragma unroll
  for (int m = 0; m < 5; ++m) { const unsigned c = 64 * m + lane; woff[m] = c * 16 + ((c * 6554) >> 16) * 1120; }
  const char* wkv = (const char*)((j & 1) ? wv : wk);
  auto stage = [&](int u) {
    if (u < 3) dma16(wkv + (w + 8 * u) * 1024, lane * 16, lb + L_R + (w + 8 * u) * 1024);
    else dma16(wqb + w * 32 * 1280, woff[u - 3], lb + L_C0 + (w * 5 + u - 3) * 1024);
  };
  u64 tag;
  asm volatile("" : "+v"(woff[0]), "+v"(woff[1]), "+v"(woff[2]), "+v"(woff[3]), "+v"(woff[4]));
  asm volatile("s_load_dwordx2 %0, %1, 0x0\n\ts_waitcnt lgkmcnt(0)\n\ts_barrier" : "=&s"(tag) : "s"(ws + W_EPOCH) : "memory");
  tag += 1;

  {
    f32x2 xf[40], a1 = {0.f, 0.f}, a2 = {0.f, 0.f}, b1 = {0.f, 0.f}, b2 = {0.f, 0.f};
#pragma unroll
    for (int i = 0; i < 10; ++i) {
      if ((i & 1) == 0)
        asm volatile("s_waitcnt vmcnt(%c6)" : "+v"(xv[i]), "+v"(xv[i + 1]), "+v"(a1), "+v"(a2), "+v"(b1), "+v"(b2) : "i"(8) : "memory");
#pragma unroll
      for (int e = 0; e < 4; ++e) {
        const unsigned u = xv[i][e];
        const f32x2 v = {__uint_as_float(u << 16), __uint_as_float(u & 0xffff0000u)};
        xf[4 * i + e] = v;
        if (e & 1) { b1 += v; b2 = __builtin_elementwise_fma(v, v, b2); } else { a1 += v; a2 = __builtin_elementwise_fma(v, v, a2); }
      }
      if ((i & 1) && i < 8) { asm volatile("" : "+v"(a1), "+v"(a2), "+v"(b1), "+v"(b2) :: "memory"); stage(i - 1); stage(i); }
      if (ISA && i == 7 && w == 3)
        touch((const char*)wq + 256 * 1280 + (b * 15 + (lane < 15 ? lane : 0)) * 128, lb + L_MISC + 1024);
      if (i == 9 && ISA && w == 0) dma16(wkv + 24 * 1024, lane * 16, lb + L_R + 24 * 1024);
    }
    a1 += b1; a2 += b2;
    float s1 = a1[0] + a1[1], s2 = a2[0] + a2[1];
    s1 += __builtin_amdgcn_update_dpp(0.f, s1, 0xB1, 0xf, 0xf, true);  s2 += __builtin_amdgcn_update_dpp(0.f, s2, 0xB1, 0xf, 0xf, true);
    s1 += __builtin_amdgcn_update_dpp(0.f, s1, 0x4E, 0xf, 0xf, true);  s2 += __builtin_amdgcn_update_dpp(0.f, s2, 0x4E, 0xf, 0xf, true);
    s1 += __builtin_amdgcn_update_dpp(0.f, s1, 0x141, 0xf, 0xf, true); s2 += __builtin_amdgcn_update_dpp(0.f, s2, 0x141, 0xf, 0xf, true);
    const float mean = s1 * (1.f / D);
    const float var = fmaxf(s2 * (1.f / D) - mean * mean, 0.f);
    const float m = rbf(mean), sd = rbf(__builtin_amdgcn_sqrtf(rbf(rbf(var) + 1e-5f)));
    const float r = __builtin_amdgcn_rcpf(sd), mr = -m * r;
    const unsigned xa = lb + L_XN + (w * 8 + (lane >> 3)) * XP + (lane & 4) * 16 + (lane & 2) * 16 + (lane & 1) * 8;
#pragma unroll
    for (int i = 0; i < 10; ++i) {
      u32x4 y;
#pragma unroll
      for (int e = 0; e < 4; ++e) { const f32x2 v = __builtin_elementwise_fma(xf[4 * i + e], (f32x2){r, r}, (f32x2){mr, mr}); y[e] = pk(v[0], v[1]); }
      *(LAS u32x2*)(uintptr_t)(xa + i * 128) = (u32x2){y[0], y[1]};
      *(LAS u32x2*)(uintptr_t)(xa + i * 128 + 16) = (u32x2){y[2], y[3]};
    }
    if (ISA) *(LAS u32x2*)(uintptr_t)(lb + L_QI + threadIdx.x * 48 + 40) = (u32x2){0u, 0u};
  }
  const unsigned xf16 = lb + L_XN + i16 * XP + g4 * 16;
  const int f0 = 16 * (g4 >> 1) + 4 * (g4 & 1) + tq;
  const unsigned wr = f0 * 160 + tp * 8;

  if constexpr (ISA) {
    const int t16 = rg * 4 + (j >> 1);
    if (w < 2) {
      const int key = (t16 & 1) * 16 + i16;
      const unsigned ko = (j & 1) ? ((w == 0 || i16 < 4) ? 1280 + (((t16 & 1) * 2 + (g4 >> 1)) * 20 + 16 * w + i16) * 16 + (g4 & 1) * 8 : 4096)
                                  : w == 0 ? ((g4 >> 1) * 32 + key) * 16 + (g4 & 1) * 8 : g4 == 0 ? 1024 + key * 8 : 4096;
      const auto krs = __builtin_amdgcn_make_buffer_rsrc((void*)(ws + W_KV + (t16 >> 1) * 2560), 0, 2560, 0x00020000);
      const unsigned kw = pin(lb + L_R + f0 * 40 + w * 32 + tp * 8), kx = pin(xf16 + (j >> 1) * 16 * XP), kop = pin(ko);
      VMWAIT(0);
      arrive(lb + L_MISC + M_R1);
      __builtin_amdgcn_s_setprio(2);
      await<8>(lb + L_MISC + M_R1);
      const f32x4 acc = kv_core(kw, kx, (j & 1));
      __builtin_amdgcn_raw_buffer_store_b64((u32x2){pk(acc[0], acc[1]), pk(acc[2], acc[3])}, krs, kop, 0, 16);
      arrive(lb + L_MISC + M_F);
      VMWAIT(0);
      if (lane == 0 && __hip_atomic_fetch_add((LAS unsigned*)(uintptr_t)(lb + L_MISC + M_KV), 1u, __ATOMIC_RELAXED, __HIP_MEMORY_SCOPE_WORKGROUP) == 1u)
        st_sc1_64(ws + W_F1 + (t16 * 2 + (j & 1)) * F1S, tag);
    } else {
      const char* tp_[2];
#pragma unroll
      for (int u = 0; u < 2; ++u) {
        unsigned ln = b * 160 + (w - 2) * 80 + u * 64 + lane; if (u == 1 && lane >= 16) ln -= 64;
        tp_[u] = ln < 17280 ? (const char*)w1 + ln * 128 : ln < 34560 ? (const char*)w2 + (ln - 17280) * 128 : (const char*)wo + (ln - 34560) * 128;
        asm volatile("" : "+v"(tp_[u]));
      }
      VMWAIT(0);
      arrive(lb + L_MISC + M_R1);
      asm volatile(R"ASM(
  s_mov_b32 s40, %6
  .set pi, 0
  .rept 6
  buffer_load_dwordx4 v[116+20*pi:119+20*pi], %0, %5, s40 offen
  buffer_load_dwordx4 v[120+20*pi:123+20*pi], %1, %5, s40 offen
  buffer_load_dwordx4 v[124+20*pi:127+20*pi], %2, %5, s40 offen
  buffer_load_dwordx4 v[128+20*pi:131+20*pi], %3, %5, s40 offen
  buffer_load_dwordx4 v[132+20*pi:135+20*pi], %4, %5, s40 offen
  .if pi & 1
  s_add_u32 s40, s40, 122880
  .else
  s_add_u32 s40, s40, 40960
  .endif
  .set pi, pi+1
  .endr
  s_mov_b32 s43, m0
  s_mov_b32 m0, %10
  s_nop 0
  global_load_lds_dword %11, off
  global_load_lds_dword %12, off
  s_mov_b32 m0, s43
  s_mov_b32 s41, 0x100000
Lf%=:
  ds_read_b32 v115, %8 offset:16
  s_waitcnt lgkmcnt(0)
  v_readfirstlane_b32 s43, v115
  s_nop 3
  s_cmp_ge_u32 s43, 6
  s_cbranch_scc1 Lg%=
  s_sub_u32 s41, s41, 1
  s_cmp_lg_u32 s41, 0
  s_cbranch_scc1 Lf%=
Lg%=:
  .set pi, 0
  .rept 6
  .if (pi>>1) == 0
  .set po, 0
  .elseif (pi>>1) == 1
  .set po, 25600
  .else
  .set po, 46080
  .endif
  .set po, po+5120*(pi&1)
  s_waitcnt vmcnt(27-5*pi)
  ds_write_b128 %7, v[116+20*pi:119+20*pi] offset:po
  ds_write_b128 %7, v[120+20*pi:123+20*pi] offset:po+1024
  ds_write_b128 %7, v[124+20*pi:127+20*pi] offset:po+2048
  ds_write_b128 %7, v[128+20*pi:131+20*pi] offset:po+3072
  ds_write_b128 %7, v[132+20*pi:135+20*pi] offset:po+4096
  .if pi & 1
  s_waitcnt lgkmcnt(0)
  s_mov_b64 exec, 1
  ds_add_u32 %8, %9 offset:4*(pi>>1)
  s_mov_b64 exec, -1
  .endif
  .set pi, pi+1
  .endr
)ASM" :: "v"(woff[0]), "v"(woff[1]), "v"(woff[2]), "v"(woff[3]), "v"(woff[4]), "s"(mk_rsrc(wqb, 640 * 1280)), "s"(((256 + (w - 2) * 64) * 1280)),
         "v"(lb + L_R + (w - 2) * 10240 + lane * 16), "v"(lb + L_MISC + M_W2), "v"(1u), "s"(lb + L_MISC + 1024), "v"(tp_[0]), "v"(tp_[1])
       : "memory", "scc", "s40", "s41", "s43", "v115", V116_235);
    }
    __builtin_amdgcn_s_setprio(0);
    const char* kvq = ws + W_KV + w * 40960;
    const u32x4 rsrc = mk_rsrc(kvq, 40960u);
    const char* flagp = ws + W_F1 + w * 64 * F1S;
#pragma nounroll
    for (unsigned thr = 4;; thr = 8) {
      unsigned redo;
      {
        const int ln = fresh_lane(), r32 = ln & 31, h = ln >> 5;
        u32x4 aw, ax;
#pragma unroll
        for (int r = 1; r < 4; ++r) { const unsigned a = lb + L_OP + (((w + r) & 3) * 6 + r - 1) * 1536; aw[r - 1] = a + ln * 16; ax[r - 1] = a + 1024 + ln * 8; }
        aw[3] = lb + L_OP + w * 9216 + ln * 16; ax[3] = lb + L_OP + w * 9216 + 1024 + ln * 8;
        const int gc = 10 * j + 2 * w + h, gl = 10 * j + 8 + (w & 1);
        const unsigned ro = (r32 >> 4) * 1024 + (r32 & 15) * 16;
        const u32x2 ao = {(gc >> 2) * 4096 + (gc & 3) * 256 + ro, w >= 2 ? 0x10000000u : (gl >> 2) * 4096 + (gl & 3) * 256 + (ln >> 4) * 1024 + (ln & 15) * 16};
        redo = attn_core(rsrc, flagp, tag, (lb + L_QI), (w * 3072), (lb + L_MISC), (thr), mk_rsrc(ws + W_O + (size_t)rg * 81920, 81920u), aw, ax, ao, lb + L_OP + 36864 + (w & 1) * 1024, (w >> 1) * 8);
      }
      if (!redo) break;
      const int lane = fresh_lane(), r32 = lane & 31, h = lane >> 5;
      {
        const char* kq = kvq;
        for (int qt = 0; qt < 8; ++qt) {
          const unsigned qa_ = lb + L_QI + qt * 1536 + r32 * 48;
          const bfx8 q1 = ld128(qa_ + h * 16);
          u32x2 q2u = *(const LAS u32x2*)(uintptr_t)(qa_ + 32);
          if (h) q2u = (u32x2){0u, 0u};
          float mx = -__builtin_inff();
          for (int kt = 0; kt < 16; ++kt) {
            const char* tp_ = kq + kt * 2560;
            u32x4 k1 = {0u, 0u, 0u, 0u};
            u64 a0 = ld_sc1_64(tp_ + lane * 16), a1 = ld_sc1_64(tp_ + lane * 16 + 8), a2 = ld_sc1_64(tp_ + 1024 + r32 * 8);
            k1 = (u32x4){(unsigned)a0, (unsigned)(a0 >> 32), (unsigned)a1, (unsigned)(a1 >> 32)};
            u32x2 k2 = {(unsigned)a2, (unsigned)(a2 >> 32)};
            f32x16 sc = {};
            sc = __builtin_amdgcn_mfma_f32_32x32x8bf16_1k(__builtin_bit_cast(s16x4v, k2), __builtin_bit_cast(s16x4v, q2u), sc, 0, 0, 0);
            sc = MFMA32(__builtin_bit_cast(bfx8, k1), q1, sc);
#pragma unroll
            for (int e = 0; e < 16; ++e) mx = fmaxf(mx, sc[e]);
          }
          auto s2 = __builtin_amdgcn_permlane32_swap(__float_as_uint(mx), __float_as_uint(mx), false, false);
          mx = fmaxf(__uint_as_float(s2[0]), __uint_as_float(s2[1]));
          if (h == 0) *(LAS float*)(uintptr_t)(lb + L_OP + ((w * 8 + qt) * 32 + r32) * 4) = mx;
        }
      }
      arrive(lb + L_MISC + M_B1); await<4>(lb + L_MISC + M_B1);
      {
        const int q = w * 64 + lane;
        float mx = -__builtin_inff();
#pragma unroll
        for (int ww = 0; ww < 4; ++ww) mx = fmaxf(mx, *(const LAS float*)(uintptr_t)(lb + L_OP + (ww * 256 + q) * 4));
        const float m0 = rbf(mx), m1 = rbf(mx - m0), m2 = rbf(mx - m0 - m1);
        *(LAS u32x2*)(uintptr_t)(lb + L_QI + q * 48 + 40) = (u32x2){pk(-m0, -m1), pk(-m2, 0.f)};
      }
      arrive(lb + L_MISC + M_B2); await<4>(lb + L_MISC + M_B2);
    }
    if (b == 0 && w == 1 && fresh_lane() == 0) st_sc1_64(ws + W_EPOCH, tag);
  } else {
    const int gw = w - 4;
    const unsigned ring = lb + L_R + gw * 16384;
    const int r32 = lane & 31, h = lane >> 5;
    u32x16 toa; u32x4 tob, toc, tod;
    {
      const int rp = gw & 1, kh = gw >> 1;
      unsigned qi[5];
#pragma unroll
      for (int t = 0; t < 5; ++t) { const int f = 16 * t + 4 * g4, head = f / 20, d = f % 20; qi[t] = pin(lb + L_QI + (head * 64 + (2 * rp + kh) * 16 + i16) * 48 + d * 2); }
      const unsigned vw = pin(lb + L_R + wr + kh * 10240), vx = pin(xf16 + rp * 32 * XP + kh * 128);
      const unsigned ex = pin(lb + L_R + gw * 5120 + lane * 16), exr = pin(lb + L_R + (gw ^ 2) * 5120 + lane * 16);
      VMWAIT(0);
      arrive(lb + L_MISC + M_R1);
      __builtin_amdgcn_s_setprio(1);
      q_core(vw, vx, ex, exr, qi[0], qi[1], qi[2], qi[3], qi[4], (lb + L_MISC), (lb + L_MISC + M_P + 4 * rp), (kh), QSCALE);
    }
    const int nun = j < 3 ? 4 : 3, unit = (j < 3 ? 4 * j : 12 + 3 * (j - 3)) + gw;
    char* hbase = ws + W_H + (size_t)rg * 54 * 4096;
    const char* wob = (const char*)wo + (size_t)gw * 160 * 1280 + j * 160;
    const int s0 = gw < 2 ? 14 * gw : 28 + 13 * (gw - 2), ns = gw < 2 ? 14 : 13;
    const char* w2b = (const char*)w2 + j * 160 + (size_t)s0 * 32 * 1280;
    const char* hb = hbase + s0 * 4096;
    if (gw < nun) {
      const unsigned voff = (lane >> 3) * 3456 + (((lane & 7) ^ (((lane >> 4) & 1) << 2)) * 16);
      const unsigned tb = ring + (4 * (g4 >> 1) + tq) * 128 + (tp & 1) * 8;
      const unsigned tr0 = tb + ((2 * (g4 & 1) + (tp >> 1)) + 4 * (tq >> 1)) * 16, tr1 = tb + ((2 * (g4 & 1) + (tp >> 1)) + 4 * (1 - (tq >> 1))) * 16;
      f32x16 acc[4];
      ffn1_core(acc, mk_rsrc((const char*)w1 + unit * 128, 640 * 3456), (ring), tr0, tr1, lb + L_XN + r32 * XP + h * 16, voff, mk_rsrc(wob, 160 * 1280), toa, tob, toc, tod, wotab.v, lane * 16,
                mk_rsrc(ws + W_O + (size_t)rg * 81920, 81920u), 10 * j + 5 * gw, gw < 2 ? g4 * 1024 + i16 * 16 : 0x10000000u);
      const auto hrs = __builtin_amdgcn_make_buffer_rsrc((void*)(hbase + unit * 8192), 0, 8192, 0x00020000);
      const unsigned ho = (r32 >> 4) * 1024 + h * 256 + (r32 & 15) * 16;
#pragma unroll
      for (int nt = 0; nt < 2; ++nt)
#pragma unroll
        for (int rt = 0; rt < 2; ++rt)
#pragma unroll
          for (int c = 0; c < 2; ++c) {
            u32x4 v;
#pragma unroll
            for (int e = 0; e < 4; ++e)
              v[e] = __builtin_bit_cast(unsigned, __builtin_elementwise_max(__builtin_bit_cast(s16x2, pk(acc[2 * nt + rt][8 * c + 2 * e], acc[2 * nt + rt][8 * c + 2 * e + 1])), (s16x2){0, 0}));
            __builtin_amdgcn_raw_buffer_store_b128(v, hrs, ho + nt * 4096 + rt * 2048 + c * 512, 0, 16);
            __builtin_amdgcn_s_sleep(2);
          }
      VMWAIT(8);
    } else {
      asm volatile("global_load_dwordx4 v[96:99], %4, %5\n\tglobal_load_dwordx4 v[100:103], %4, %5 offset:1024\n\tglobal_load_dwordx4 v[104:107], %4, %5 offset:2048\n\t"
                   "global_load_dwordx4 v[108:111], %4, %5 offset:3072\n\tglobal_load_dwordx4 v[112:115], %6, %5\n\tglobal_load_dwordx4 v[116:119], %6, %5 offset:1024\n\t"
                   "global_load_dwordx4 v[120:123], %6, %5 offset:2048\n\ts_waitcnt vmcnt(0)"
                   : "=&{v[96:111]}"(toa), "=&{v[112:115]}"(tob), "=&{v[116:119]}"(toc), "=&{v[120:123]}"(tod) : "v"(lane * 16), "s"(wotab.v), "v"(lane * 16 + 4096) : "memory");
#pragma unroll
      for (int m = 0; m < 15; ++m) dma16(wob, toa[m], ring + m * 1024);
      VMWAIT(0);
    }
    arrive(lb + L_MISC + M_F1);
    __builtin_amdgcn_s_setprio(1);
    WoF wf;
    const unsigned toa15 = toa[15];
    wo_frags_a(wf, ring + wr);
    VMWAIT(0);
    if (gw < nun && lane == 0) st_sc1_64(ws + W_F2 + (rg * 32 + unit) * 128, tag);
    dma16(wob, toa15, ring + 5120);
#pragma unroll
    for (int m = 0; m < 4; ++m) dma16(wob, tob[m], ring + 5120 + (m + 1) * 1024);
#pragma unroll
    for (int m = 0; m < 4; ++m) dma16(wob, toc[m], ring + 10240 + m * 1024);
    dma16(wob, tod[0], ring + 10240 + 4096);
#pragma unroll
    for (int m = 0; m < 5; ++m) dma16(w2b, woff[m], ring + m * 1024);
    VMWAIT(5);
    wo_frags_b(wf, ring + wr);
#pragma unroll
    for (int m = 0; m < 5; ++m) dma16(w2b + 32 * 1280, woff[m], ring + 5120 + m * 1024);
    poll(ws + W_F2 + (rg * 32 + (s0 >> 1) + (lane < 7 ? lane : 0)) * 128, lane < 7, tag);
    VMWAIT(0);
    __builtin_amdgcn_s_setprio(0);
    f32x16 a5[5];
    ffn2_core(a5, mk_rsrc(w2b, ns * 40960), mk_rsrc(hb, ns * 4096), ring, ns, ring + wr,
              woff[0], woff[1] - 1024, woff[2] - 2048, woff[3] - 3072, woff[4], lane * 16);
    {
      const int ln = fresh_lane();
      const size_t ob = ((size_t)(rg * 64 + gw * 16) * D + j * 80) * 2;
      wo_core(a5, wf, mk_rsrc(ws + W_O + (size_t)rg * 81920 + gw * 20480, 20480u), mk_rsrc((const char*)x + ob, 16 * 1280), mk_rsrc((char*)out + ob, 16 * 1280),
              gw, lb + L_MISC + M_E1, ln * 16, lb + L_R + ln * 16, (ln & 15) * 1280 + (ln >> 4) * 8);
    }
  }
}

__global__ __launch_bounds__(512, 2) void fused_kernel(const u16* __restrict__ x, const u16* __restrict__ wq, const u16* __restrict__ wk,
                                                       const u16* __restrict__ wv, const u16* __restrict__ wo, const u16* __restrict__ w1,
                                                       const u16* __restrict__ w2, u16* __restrict__ out, char* __restrict__ ws) {
  extern __shared__ __attribute__((aligned(16))) char smem[];
  const unsigned lb = (unsigned)(uintptr_t)smem;
  const int w = rfl(threadIdx.x >> 6), lane = threadIdx.x & 63;
  const int b = blockIdx.x;
  {
    u64 pc; asm volatile("s_getpc_b64 %0" : "=s"(pc));
    const unsigned ln = w * 40 + (lane < 40 ? lane : 39);
    if (((b >> 3) & 3) == 0) touch((const char*)((pc & ~127ull) + (ln < CODE_LINES ? ln : CODE_LINES - 1) * 128), lb + L_MISC + 1024);
    const char* xb_ = (const char*)x + (size_t)(row_group(b) * 64 + w * 8) * 1280;
    if (threadIdx.x < 32) *(LAS unsigned*)(uintptr_t)(lb + L_MISC + threadIdx.x * 4) = 0u;
    asm volatile("global_load_dwordx4 v[200:203], %0, %1\n\tglobal_load_dwordx4 v[204:207], %0, %1 offset:128\n\tglobal_load_dwordx4 v[208:211], %0, %1 offset:256\n\t"
                 "global_load_dwordx4 v[212:215], %0, %1 offset:384\n\tglobal_load_dwordx4 v[216:219], %0, %1 offset:512\n\tglobal_load_dwordx4 v[220:223], %0, %1 offset:640\n\t"
                 "global_load_dwordx4 v[224:227], %0, %1 offset:768\n\tglobal_load_dwordx4 v[228:231], %0, %1 offset:896\n\tglobal_load_dwordx4 v[232:235], %0, %1 offset:1024\n\t"
                 "global_load_dwordx4 v[236:239], %0, %1 offset:1152"
                 :: "v"((lane >> 3) * 1280 + (lane & 7) * 16), "s"(xb_) : "memory", V200_239);
  }
  if (w < 4) role<true>(x, wq, wk, wv, wo, w1, w2, out, ws, w);
  else role<false>(x, wq, wk, wv, wo, w1, w2, out, ws, w);
}
}

extern "C" void kernel_launch(void* const* d_in, const int* in_sizes, int n_in, void* d_out, int out_size, void* d_ws, size_t ws_size,
                              hipStream_t stream) {
  (void)in_sizes; (void)n_in; (void)out_size; (void)ws_size;
  (void)hipFuncSetAttribute((const void*)fused_kernel, hipFuncAttributeMaxDynamicSharedMemorySize, L_TOTAL);
  fused_kernel<<<256, 512, L_TOTAL, stream>>>((const u16*)d_in[0], (const u16*)d_in[1], (const u16*)d_in[2], (const u16*)d_in[3],
                                              (const u16*)d_in[4], (const u16*)d_in[6], (const u16*)d_in[7], (u16*)d_out, (char*)d_ws);
}
```
